```python
import torch

def _softmax(x, axis=-1):
    return torch.softmax(x, dim=axis)

def _gelu(x):
    return torch.nn.functional.gelu(x, approximate='tanh')

def _silu(x):
    return torch.nn.functional.silu(x)

def _sigmoid(x):
    return torch.sigmoid(x)

def _layernorm(x, axis=-1, eps=1e-05):
    m = x.mean(dim=axis, keepdim=True)
    v = x.var(dim=axis, keepdim=True, unbiased=False)
    return (x - m) / torch.sqrt(v + eps)

def _scatter_add(tgt, idx, src, axis=0):
    return tgt.clone().index_add_(axis, idx, src)

def _segment_sum(src, seg, n):
    out = torch.zeros((n,) + src.shape[1:], dtype=src.dtype)
    return out.index_add_(0, seg, src)

def _pad(x, pw):
    flat = tuple((v for pair in reversed(pw) for v in pair))
    return torch.nn.functional.pad(x, flat)
kernel_name = 'adaLN_cond__sliding_block_cached__dense_activation_relu__decode'

def setup_inputs():

    _w = lambda a: (lambda t: t.to(torch.bfloat16) if t.is_floating_point() else t)(torch.as_tensor(a))

    def _A_setup():
        B, Sq, D = (1, 1, 640)
        Hq, Hkv, Sk, Dh = (32, 32, 16384, 128)
        assert Hq % Hkv == 0, (Hq, Hkv)
        return {'x': _w((lambda *s: torch.randn(*s, dtype=torch.bfloat16) * 0.05)(B, Sq, D)), 'k_cache': _w((lambda *s: torch.randn(*s, dtype=torch.bfloat16) * 3.2)(B, Hkv, Sk, Dh)), 'v_cache': _w((lambda *s: torch.randn(*s, dtype=torch.bfloat16) * 0.4)(B, Hkv, Sk, Dh)), 'wq': _w((lambda *s: torch.randn(*s, dtype=torch.bfloat16) * 3.2)(D, Hq * Dh) * D ** (-0.5)), 'wo': _w((lambda *s: torch.randn(*s, dtype=torch.bfloat16) * 0.4)(Hq * Dh, D) * (Hq * Dh) ** (-0.5))}
    _A_in = _A_setup()

    def _B_setup():
        B, S, D, Dff = (1, 1, 640, 1728)
        return {'x': _w((lambda *s: torch.randn(*s, dtype=torch.bfloat16) * 0.05)(B, S, D)), 'w1': _w((lambda *s: torch.randn(*s, dtype=torch.bfloat16) * 0.05)(D, Dff)), 'w2': _w((lambda *s: torch.randn(*s, dtype=torch.bfloat16) * 0.05)(Dff, D))}
    _B_in = _B_setup()
    return {'A_x': _A_in['x'], 'A_k_cache': _A_in['k_cache'], 'A_v_cache': _A_in['v_cache'], 'A_wq': _A_in['wq'], 'A_wo': _A_in['wo'], 'B_x': _B_in['x'], 'B_w1': _B_in['w1'], 'B_w2': _B_in['w2'], 'c': _w((lambda *s: torch.randn(*s, dtype=torch.bfloat16) * 0.1)(1, 640) * 8.0), 'w_mod': _w((lambda *s: torch.randn(*s, dtype=torch.bfloat16) * 0.1)(640, 6 * 640) * 640 ** (-0.5) * 8.0)}

def reference(A_x, A_k_cache, A_v_cache, A_wq, A_wo, B_x, B_w1, B_w2, c, w_mod):

    def _A_ref(x, k_cache, v_cache, wq, wo):
        B, Sq, D = x.shape
        Hq, Dh = (32, 128)
        Hkv, Sk = (k_cache.shape[1], k_cache.shape[2])
        W = min(4096, max(256, Sk // 4))
        rep = Hq // Hkv
        q = (lambda x, *d: x.permute(*d))((x @ wq).reshape(B, Sq, Hq, Dh), 0, 2, 1, 3)
        k = (lambda x, n, axis: torch.repeat_interleave(x, n, dim=axis))(k_cache[:, :, Sk - W:, :], rep, axis=1)
        v = (lambda x, n, axis: torch.repeat_interleave(x, n, dim=axis))(v_cache[:, :, Sk - W:, :], rep, axis=1)
        scores = torch.einsum('bhqd,bhkd->bhqk', q, k) * Dh ** (-0.5)
        p = _softmax(scores, axis=-1)
        o = torch.einsum('bhqk,bhkd->bhqd', p, v)
        return (lambda x, *d: x.permute(*d))(o, 0, 2, 1, 3).reshape(B, Sq, Hq * Dh) @ wo

    def _B_ref(x, w1, w2):
        h = torch.einsum('bsd,df->bsf', x, w1)
        zero = torch.zeros_like(h)
        h = torch.maximum(h, zero)
        return torch.einsum('bsf,fd->bsd', h, w2)
    x = A_x
    mod = (_silu(c) @ w_mod).reshape(c.shape[0], 6, 1, -1)
    sa, ca, ga, sb, cb, gb = (mod[:, 0].clone(), mod[:, 1].clone(), mod[:, 2].clone(), mod[:, 3].clone(), mod[:, 4].clone(), mod[:, 5].clone())
    h = _layernorm(x, axis=-1) * (1 + ca) + sa
    x = x + ga * _A_ref(h, A_k_cache, A_v_cache, A_wq, A_wo)
    h = _layernorm(x, axis=-1) * (1 + cb) + sb
    x = x + gb * _B_ref(h, B_w1, B_w2)
    return x
```

```cpp
#include <hip/hip_runtime.h>
#include <hip/hip_bf16.h>
#include <stdint.h>

typedef unsigned short u16;
typedef unsigned v4u __attribute__((ext_vector_type(4)));
typedef __bf16 bf16x2 __attribute__((ext_vector_type(2)));

constexpr int D = 640, HQ = 32, SK = 16384, DH = 128, W = 4096, DFF = 1728;
constexpr int NSPLIT = 8;

__device__ __forceinline__ float bf2f(u16 b) { return __uint_as_float((unsigned)b << 16); }
__device__ __forceinline__ float rbf(float f) { return (float)(__bf16)f; }
__device__ __forceinline__ float lo(unsigned u) { return __uint_as_float(u << 16); }
__device__ __forceinline__ float hi(unsigned u) { return __uint_as_float(u & 0xffff0000u); }

template <int K, int N>
__device__ __forceinline__ void gemv_block(const float* sx, const u16* Wt, float* sred, float& o0, float& o1) {
  const int tid = threadIdx.x, cp = tid & 31, ks = tid >> 5;
  const int n0 = blockIdx.x * 64 + cp * 2;
  float a0 = 0.f, a1 = 0.f;
  #pragma unroll 8
  for (int k = ks; k < K; k += 8) {
    unsigned w = *(const unsigned*)(Wt + (size_t)k * N + n0);
    float x = sx[k];
    a0 = fmaf(x, lo(w), a0); a1 = fmaf(x, hi(w), a1);
  }
  sred[(ks * 32 + cp) * 2] = a0; sred[(ks * 32 + cp) * 2 + 1] = a1;
  __syncthreads();
  o0 = 0.f; o1 = 0.f;
  if (tid < 32) { for (int s = 0; s < 8; ++s) { o0 += sred[(s * 32 + tid) * 2]; o1 += sred[(s * 32 + tid) * 2 + 1]; } }
}

__device__ __forceinline__ void block_ln(const float* sx_in, float* sred, float& mean, float& rstd) {
  const int tid = threadIdx.x;
  float s = 0.f;
  for (int i = tid; i < D; i += 256) s += sx_in[i];
  for (int o = 32; o; o >>= 1) s += __shfl_xor(s, o);
  if ((tid & 63) == 0) sred[tid >> 6] = s;
  __syncthreads();
  mean = (sred[0] + sred[1] + sred[2] + sred[3]) * (1.f / D);
  __syncthreads();
  float v = 0.f;
  for (int i = tid; i < D; i += 256) { float d = sx_in[i] - mean; v += d * d; }
  for (int o = 32; o; o >>= 1) v += __shfl_xor(v, o);
  if ((tid & 63) == 0) sred[tid >> 6] = v;
  __syncthreads();
  float var = (sred[0] + sred[1] + sred[2] + sred[3]) * (1.f / D);
  rstd = 1.f / sqrtf(var + 1e-5f);
  __syncthreads();
}

__global__ void __launch_bounds__(256) k_mod(const u16* c, const u16* wmod, float* mod) {
  __shared__ float sx[D]; __shared__ float sred[512];
  for (int i = threadIdx.x; i < D; i += 256) { float v = bf2f(c[i]); sx[i] = rbf(v / (1.f + __expf(-v))); }
  __syncthreads();
  float o0, o1; gemv_block<D, 6 * D>(sx, wmod, sred, o0, o1);
  if (threadIdx.x < 32) { int n0 = blockIdx.x * 64 + threadIdx.x * 2; mod[n0] = rbf(o0); mod[n0 + 1] = rbf(o1); }
}
__global__ void __launch_bounds__(256) k_q(const u16* x, const float* mod, const u16* wq, float* q) {
  __shared__ float sx[D]; __shared__ float sred[512];
  for (int i = threadIdx.x; i < D; i += 256) sx[i] = bf2f(x[i]);
  __syncthreads();
  float mean, rstd; block_ln(sx, sred, mean, rstd);
  for (int i = threadIdx.x; i < D; i += 256) sx[i] = rbf((sx[i] - mean) * rstd * (1.f + mod[D + i]) + mod[i]);
  __syncthreads();
  float o0, o1; gemv_block<D, HQ * DH>(sx, wq, sred, o0, o1);
  if (threadIdx.x < 32) { int n0 = blockIdx.x * 64 + threadIdx.x * 2; q[n0] = rbf(o0); q[n0 + 1] = rbf(o1); }
}
__global__ void __launch_bounds__(256) k_attn(const float* q, const u16* kc, const u16* vc, float* pml, float* po) {
  __shared__ float ss[512]; __shared__ float sred[16]; __shared__ float sacc[16][DH];
  const int s = blockIdx.x, h = blockIdx.y, tid = threadIdx.x, lane = tid & 63, wave = tid >> 6, r = lane >> 4, c = lane & 15;
  const u16* kb = kc + ((size_t)h * SK + (SK - W) + s * 512) * DH;
  const u16* vb = vc + ((size_t)h * SK + (SK - W) + s * 512) * DH;
  float qv[8];
  for (int j = 0; j < 8; ++j) qv[j] = q[h * DH + c * 8 + j];
  #pragma unroll 8
  for (int i = 0; i < 32; ++i) {
    int key = (i * 4 + wave) * 4 + r;
    v4u kk = *(const v4u*)(kb + (size_t)key * DH + c * 8);
    float d = lo(kk.x) * qv[0] + hi(kk.x) * qv[1] + lo(kk.y) * qv[2] + hi(kk.y) * qv[3] + lo(kk.z) * qv[4] + hi(kk.z) * qv[5] + lo(kk.w) * qv[6] + hi(kk.w) * qv[7];
    d += __shfl_xor(d, 1); d += __shfl_xor(d, 2); d += __shfl_xor(d, 4); d += __shfl_xor(d, 8);
    if (c == 0) ss[key] = d * 0.08838834764831845f;
  }
  __syncthreads();
  float m = fmaxf(ss[tid], ss[tid + 256]);
  for (int o = 32; o; o >>= 1) m = fmaxf(m, __shfl_xor(m, o));
  if (lane == 0) sred[wave] = m;
  __syncthreads();
  m = fmaxf(fmaxf(sred[0], sred[1]), fmaxf(sred[2], sred[3]));
  float p0 = __expf(ss[tid] - m), p1 = __expf(ss[tid + 256] - m);
  ss[tid] = p0; ss[tid + 256] = p1;
  float l = p0 + p1;
  for (int o = 32; o; o >>= 1) l += __shfl_xor(l, o);
  if (lane == 0) sred[4 + wave] = l;
  __syncthreads();
  l = sred[4] + sred[5] + sred[6] + sred[7];
  float acc[8] = {0, 0, 0, 0, 0, 0, 0, 0};
  #pragma unroll 8
  for (int i = 0; i < 32; ++i) {
    int key = (i * 4 + wave) * 4 + r;
    v4u vv = *(const v4u*)(vb + (size_t)key * DH + c * 8);
    float p = ss[key];
    acc[0] += p * lo(vv.x); acc[1] += p * hi(vv.x); acc[2] += p * lo(vv.y); acc[3] += p * hi(vv.y);
    acc[4] += p * lo(vv.z); acc[5] += p * hi(vv.z); acc[6] += p * lo(vv.w); acc[7] += p * hi(vv.w);
  }
  for (int j = 0; j < 8; ++j) sacc[wave * 4 + r][c * 8 + j] = acc[j];
  __syncthreads();
  if (tid < DH) { float a = 0.f; for (int g = 0; g < 16; ++g) a += sacc[g][tid]; po[(h * NSPLIT + s) * DH + tid] = a; }
  if (tid == 0) { pml[(h * NSPLIT + s) * 2] = m; pml[(h * NSPLIT + s) * 2 + 1] = l; }
}
__global__ void __launch_bounds__(256) k_wo(const float* pml, const float* po, const u16* wo, float* part) {
  __shared__ float sx[DH]; __shared__ float sred[512];
  const int h = blockIdx.y, tid = threadIdx.x;
  if (tid < DH) {
    float M = -INFINITY; for (int s = 0; s < NSPLIT; ++s) M = fmaxf(M, pml[(h * NSPLIT + s) * 2]);
    float L = 0.f, o = 0.f;
    for (int s = 0; s < NSPLIT; ++s) { float w = __expf(pml[(h * NSPLIT + s) * 2] - M); L += w * pml[(h * NSPLIT + s) * 2 + 1]; o += w * po[(h * NSPLIT + s) * DH + tid]; }
    sx[tid] = rbf(o / L);
  }
  __syncthreads();
  float o0, o1; gemv_block<DH, D>(sx, wo + (size_t)h * DH * D, sred, o0, o1);
  if (tid < 32) { int n0 = blockIdx.x * 64 + tid * 2; part[h * D + n0] = o0; part[h * D + n0 + 1] = o1; }
}
__global__ void __launch_bounds__(256) k_w1(const u16* x, const float* mod, const float* part, const u16* w1, float* hid, float* x1out) {
  __shared__ float sx[D]; __shared__ float sred[512];
  for (int i = threadIdx.x; i < D; i += 256) { float a = 0.f; for (int h = 0; h < HQ; ++h) a += part[h * D + i]; float v = rbf(bf2f(x[i]) + rbf(mod[2 * D + i] * rbf(a))); sx[i] = v; if (blockIdx.x == 0) x1out[i] = v; }
  __syncthreads();
  float mean, rstd; block_ln(sx, sred, mean, rstd);
  for (int i = threadIdx.x; i < D; i += 256) sx[i] = rbf((sx[i] - mean) * rstd * (1.f + mod[4 * D + i]) + mod[3 * D + i]);
  __syncthreads();
  float o0, o1; gemv_block<D, DFF>(sx, w1, sred, o0, o1);
  if (threadIdx.x < 32) { int n0 = blockIdx.x * 64 + threadIdx.x * 2; hid[n0] = fmaxf(rbf(o0), 0.f); hid[n0 + 1] = fmaxf(rbf(o1), 0.f); }
}
__global__ void __launch_bounds__(256) k_w2(const float* hid, const float* x1, const float* mod, const u16* w2, __hip_bfloat16* out) {
  __shared__ float sx[DFF]; __shared__ float sred[512];
  for (int i = threadIdx.x; i < DFF; i += 256) sx[i] = hid[i];
  __syncthreads();
  float o0, o1; gemv_block<DFF, D>(sx, w2, sred, o0, o1);
  if (threadIdx.x < 32) { int n0 = blockIdx.x * 64 + threadIdx.x * 2;
    out[n0] = __float2bfloat16(x1[n0] + rbf(mod[5 * D + n0] * rbf(o0))); out[n0 + 1] = __float2bfloat16(x1[n0 + 1] + rbf(mod[5 * D + n0 + 1] * rbf(o1))); }
}

extern "C" void kernel_launch(void* const* d_in, const int* in_sizes, int n_in, void* d_out, int out_size, void* d_ws, size_t ws_size, hipStream_t stream) {
  const u16* x = (const u16*)d_in[0]; const u16* kc = (const u16*)d_in[1]; const u16* vc = (const u16*)d_in[2];
  const u16* wq = (const u16*)d_in[3]; const u16* wo = (const u16*)d_in[4]; const u16* w1 = (const u16*)d_in[6]; const u16* w2 = (const u16*)d_in[7];
  const u16* c = (const u16*)d_in[8]; const u16* wmod = (const u16*)d_in[9];
  float* ws = (float*)d_ws;
  float* mod = ws; float* q = ws + 4096; float* pml = ws + 8192; float* po = ws + 12288; float* part = po + HQ * NSPLIT * DH; float* hid = part + HQ * D; float* x1 = hid + 2048;
  k_mod<<<6 * D / 64, 256, 0, stream>>>(c, wmod, mod);
  k_q<<<HQ * DH / 64, 256, 0, stream>>>(x, mod, wq, q);
  k_attn<<<dim3(NSPLIT, HQ), 256, 0, stream>>>(q, kc, vc, pml, po);
  k_wo<<<dim3(D / 64, HQ), 256, 0, stream>>>(pml, po, wo, part);
  k_w1<<<DFF / 64, 256, 0, stream>>>(x, mod, part, w1, hid, x1);
  k_w2<<<D / 64, 256, 0, stream>>>(hid, x1, mod, w2, (__hip_bfloat16*)d_out);
}
```

```cpp
#include <hip/hip_runtime.h>
#include <hip/hip_bf16.h>
#include <stdint.h>

typedef unsigned short u16;
typedef unsigned v4u __attribute__((ext_vector_type(4)));
typedef __bf16 bf16x2 __attribute__((ext_vector_type(2)));

constexpr int D = 640, HQ = 32, SK = 16384, DH = 128, W = 4096, DFF = 1728;
constexpr int NSPLIT = 8;

__device__ __forceinline__ float bf2f(u16 b) { return __uint_as_float((unsigned)b << 16); }
__device__ __forceinline__ float rbf(float f) { return (float)(__bf16)f; }
__device__ __forceinline__ float lo(unsigned u) { return __uint_as_float(u << 16); }
__device__ __forceinline__ float hi(unsigned u) { return __uint_as_float(u & 0xffff0000u); }

template <int K, int N>
__device__ __forceinline__ void gemv_block(const float* sx, const u16* Wt, float* sred, float& o0, float& o1) {
  const int tid = threadIdx.x, cp = tid & 31, ks = tid >> 5;
  const int n0 = blockIdx.x * 64 + cp * 2;
  float a0 = 0.f, a1 = 0.f;
  #pragma unroll 8
  for (int k = ks; k < K; k += 8) {
    unsigned w = *(const unsigned*)(Wt + (size_t)k * N + n0);
    float x = sx[k];
    a0 = fmaf(x, lo(w), a0); a1 = fmaf(x, hi(w), a1);
  }
  sred[(ks * 32 + cp) * 2] = a0; sred[(ks * 32 + cp) * 2 + 1] = a1;
  __syncthreads();
  o0 = 0.f; o1 = 0.f;
  if (tid < 32) { for (int s = 0; s < 8; ++s) { o0 += sred[(s * 32 + tid) * 2]; o1 += sred[(s * 32 + tid) * 2 + 1]; } }
}

__device__ __forceinline__ void block_ln(const float* sx_in, float* sred, float& mean, float& rstd) {
  const int tid = threadIdx.x;
  float s = 0.f;
  for (int i = tid; i < D; i += 256) s += sx_in[i];
  for (int o = 32; o; o >>= 1) s += __shfl_xor(s, o);
  if ((tid & 63) == 0) sred[tid >> 6] = s;
  __syncthreads();
  mean = (sred[0] + sred[1] + sred[2] + sred[3]) * (1.f / D);
  __syncthreads();
  float v = 0.f;
  for (int i = tid; i < D; i += 256) { float d = sx_in[i] - mean; v += d * d; }
  for (int o = 32; o; o >>= 1) v += __shfl_xor(v, o);
  if ((tid & 63) == 0) sred[tid >> 6] = v;
  __syncthreads();
  float var = (sred[0] + sred[1] + sred[2] + sred[3]) * (1.f / D);
  rstd = rbf(sqrtf(rbf(rbf(var) + 1e-5f)));
  mean = rbf(mean);
  __syncthreads();
}

__global__ void __launch_bounds__(256) k_mod(const u16* c, const u16* wmod, float* mod) {
  __shared__ float sx[D]; __shared__ float sred[512];
  for (int i = threadIdx.x; i < D; i += 256) { float v = bf2f(c[i]); sx[i] = rbf(v / (1.f + __expf(-v))); }
  __syncthreads();
  float o0, o1; gemv_block<D, 6 * D>(sx, wmod, sred, o0, o1);
  if (threadIdx.x < 32) { int n0 = blockIdx.x * 64 + threadIdx.x * 2; mod[n0] = rbf(o0); mod[n0 + 1] = rbf(o1); }
}
__global__ void __launch_bounds__(256) k_q(const u16* x, const float* mod, const u16* wq, float* q) {
  __shared__ float sx[D]; __shared__ float sred[512];
  for (int i = threadIdx.x; i < D; i += 256) sx[i] = bf2f(x[i]);
  __syncthreads();
  float mean, rstd; block_ln(sx, sred, mean, rstd);
  for (int i = threadIdx.x; i < D; i += 256) sx[i] = rbf(rbf(rbf(rbf(sx[i] - mean) / rstd) * rbf(1.f + mod[D + i])) + mod[i]);
  __syncthreads();
  float o0, o1; gemv_block<D, HQ * DH>(sx, wq, sred, o0, o1);
  if (threadIdx.x < 32) { int n0 = blockIdx.x * 64 + threadIdx.x * 2; q[n0] = rbf(o0); q[n0 + 1] = rbf(o1); }
}
__global__ void __launch_bounds__(256) k_attn(const float* q, const u16* kc, const u16* vc, float* pml, float* po) {
  __shared__ float ss[512]; __shared__ float sred[16]; __shared__ float sacc[16][DH];
  const int s = blockIdx.x, h = blockIdx.y, tid = threadIdx.x, lane = tid & 63, wave = tid >> 6, r = lane >> 4, c = lane & 15;
  const u16* kb = kc + ((size_t)h * SK + (SK - W) + s * 512) * DH;
  const u16* vb = vc + ((size_t)h * SK + (SK - W) + s * 512) * DH;
  float qv[8];
  for (int j = 0; j < 8; ++j) qv[j] = q[h * DH + c * 8 + j];
  #pragma unroll 8
  for (int i = 0; i < 32; ++i) {
    int key = (i * 4 + wave) * 4 + r;
    v4u kk = *(const v4u*)(kb + (size_t)key * DH + c * 8);
    float d = lo(kk.x) * qv[0] + hi(kk.x) * qv[1] + lo(kk.y) * qv[2] + hi(kk.y) * qv[3] + lo(kk.z) * qv[4] + hi(kk.z) * qv[5] + lo(kk.w) * qv[6] + hi(kk.w) * qv[7];
    d += __shfl_xor(d, 1); d += __shfl_xor(d, 2); d += __shfl_xor(d, 4); d += __shfl_xor(d, 8);
    if (c == 0) ss[key] = rbf(rbf(d) * 0.08838834764831845f);
  }
  __syncthreads();
  float m = fmaxf(ss[tid], ss[tid + 256]);
  for (int o = 32; o; o >>= 1) m = fmaxf(m, __shfl_xor(m, o));
  if (lane == 0) sred[wave] = m;
  __syncthreads();
  m = fmaxf(fmaxf(sred[0], sred[1]), fmaxf(sred[2], sred[3]));
  float p0 = __expf(ss[tid] - m), p1 = __expf(ss[tid + 256] - m);
  ss[tid] = p0; ss[tid + 256] = p1;
  float l = p0 + p1;
  for (int o = 32; o; o >>= 1) l += __shfl_xor(l, o);
  if (lane == 0) sred[4 + wave] = l;
  __syncthreads();
  l = sred[4] + sred[5] + sred[6] + sred[7];
  float acc[8] = {0, 0, 0, 0, 0, 0, 0, 0};
  #pragma unroll 8
  for (int i = 0; i < 32; ++i) {
    int key = (i * 4 + wave) * 4 + r;
    v4u vv = *(const v4u*)(vb + (size_t)key * DH + c * 8);
    float p = ss[key];
    acc[0] += p * lo(vv.x); acc[1] += p * hi(vv.x); acc[2] += p * lo(vv.y); acc[3] += p * hi(vv.y);
    acc[4] += p * lo(vv.z); acc[5] += p * hi(vv.z); acc[6] += p * lo(vv.w); acc[7] += p * hi(vv.w);
  }
  for (int j = 0; j < 8; ++j) sacc[wave * 4 + r][c * 8 + j] = acc[j];
  __syncthreads();
  if (tid < DH) { float a = 0.f; for (int g = 0; g < 16; ++g) a += sacc[g][tid]; po[(h * NSPLIT + s) * DH + tid] = a; }
  if (tid == 0) { pml[(h * NSPLIT + s) * 2] = m; pml[(h * NSPLIT + s) * 2 + 1] = l; }
}
__global__ void __launch_bounds__(256) k_wo(const float* pml, const float* po, const u16* wo, float* part) {
  __shared__ float sx[DH]; __shared__ float sred[512];
  const int h = blockIdx.y, tid = threadIdx.x;
  if (tid < DH) {
    float M = -INFINITY; for (int s = 0; s < NSPLIT; ++s) M = fmaxf(M, pml[(h * NSPLIT + s) * 2]);
    float L = 0.f, o = 0.f;
    for (int s = 0; s < NSPLIT; ++s) { float w = __expf(pml[(h * NSPLIT + s) * 2] - M); L += w * pml[(h * NSPLIT + s) * 2 + 1]; o += w * po[(h * NSPLIT + s) * DH + tid]; }
    sx[tid] = o / L;
  }
  __syncthreads();
  float o0, o1; gemv_block<DH, D>(sx, wo + (size_t)h * DH * D, sred, o0, o1);
  if (tid < 32) { int n0 = blockIdx.x * 64 + tid * 2; part[h * D + n0] = o0; part[h * D + n0 + 1] = o1; }
}
__global__ void __launch_bounds__(256) k_w1(const u16* x, const float* mod, const float* part, const u16* w1, float* hid, float* x1out) {
  __shared__ float sx[D]; __shared__ float sred[512];
  for (int i = threadIdx.x; i < D; i += 256) { float a = 0.f; for (int h = 0; h < HQ; ++h) a += part[h * D + i]; float v = rbf(bf2f(x[i]) + rbf(mod[2 * D + i] * rbf(a))); sx[i] = v; if (blockIdx.x == 0) x1out[i] = v; }
  __syncthreads();
  float mean, rstd; block_ln(sx, sred, mean, rstd);
  for (int i = threadIdx.x; i < D; i += 256) sx[i] = rbf((sx[i] - mean) / rstd * (1.f + mod[4 * D + i]) + mod[3 * D + i]);
  __syncthreads();
  float o0, o1; gemv_block<D, DFF>(sx, w1, sred, o0, o1);
  if (threadIdx.x < 32) { int n0 = blockIdx.x * 64 + threadIdx.x * 2; hid[n0] = fmaxf(rbf(o0), 0.f); hid[n0 + 1] = fmaxf(rbf(o1), 0.f); }
}
__global__ void __launch_bounds__(256) k_w2(const float* hid, const float* x1, const float* mod, const u16* w2, __hip_bfloat16* out) {
  __shared__ float sx[DFF]; __shared__ float sred[512];
  for (int i = threadIdx.x; i < DFF; i += 256) sx[i] = hid[i];
  __syncthreads();
  float o0, o1; gemv_block<DFF, D>(sx, w2, sred, o0, o1);
  if (threadIdx.x < 32) { int n0 = blockIdx.x * 64 + threadIdx.x * 2;
    out[n0] = __float2bfloat16(x1[n0] + rbf(mod[5 * D + n0] * rbf(o0))); out[n0 + 1] = __float2bfloat16(x1[n0 + 1] + rbf(mod[5 * D + n0 + 1] * rbf(o1))); }
}

extern "C" void kernel_launch(void* const* d_in, const int* in_sizes, int n_in, void* d_out, int out_size, void* d_ws, size_t ws_size, hipStream_t stream) {
  const u16* x = (const u16*)d_in[0]; const u16* kc = (const u16*)d_in[1]; const u16* vc = (const u16*)d_in[2];
  const u16* wq = (const u16*)d_in[3]; const u16* wo = (const u16*)d_in[4]; const u16* w1 = (const u16*)d_in[6]; const u16* w2 = (const u16*)d_in[7];
  const u16* c = (const u16*)d_in[8]; const u16* wmod = (const u16*)d_in[9];
  float* ws = (float*)d_ws;
  float* mod = ws; float* q = ws + 4096; float* pml = ws + 8192; float* po = ws + 12288; float* part = po + HQ * NSPLIT * DH; float* hid = part + HQ * D; float* x1 = hid + 2048;
  k_mod<<<6 * D / 64, 256, 0, stream>>>(c, wmod, mod);
  k_q<<<HQ * DH / 64, 256, 0, stream>>>(x, mod, wq, q);
  k_attn<<<dim3(NSPLIT, HQ), 256, 0, stream>>>(q, kc, vc, pml, po);
  k_wo<<<dim3(D / 64, HQ), 256, 0, stream>>>(pml, po, wo, part);
  k_w1<<<DFF / 64, 256, 0, stream>>>(x, mod, part, w1, hid, x1);
  k_w2<<<D / 64, 256, 0, stream>>>(hid, x1, mod, w2, (__hip_bfloat16*)d_out);
}
```

```cpp
#define KV_W1 4
#define KV_N1 11
#define KV_WIN 6
#include <hip/hip_runtime.h>
#include <hip/hip_bf16.h>
#include <stdint.h>

typedef unsigned short u16;
typedef unsigned v4u __attribute__((ext_vector_type(4)));
typedef unsigned v2u __attribute__((ext_vector_type(2)));
typedef float f32x4 __attribute__((ext_vector_type(4)));
typedef float f32x2 __attribute__((ext_vector_type(2)));
typedef __bf16 bf16x8 __attribute__((ext_vector_type(8)));
typedef __bf16 bf16x2 __attribute__((ext_vector_type(2)));
#define LAS __attribute__((address_space(3)))
#define DI __device__ __forceinline__
constexpr unsigned SENT = 0xFFFFFFFFu;
constexpr int SPIN = 200000;
constexpr int OOB = 0x7fffff00;
DI float bflo(unsigned u) { return __uint_as_float(u << 16); }
DI float bfhi(unsigned u) { return __uint_as_float(u & 0xffff0000u); }
DI unsigned cvtpk(float lo, float hi) { bf16x2 v = {(__bf16)lo, (__bf16)hi}; return __builtin_bit_cast(unsigned, v); }
DI float rbf(float f) { return (float)(__bf16)f; }
DI f32x4 mfma(v4u a, v4u b, f32x4 c) { return __builtin_amdgcn_mfma_f32_16x16x32_bf16(__builtin_bit_cast(bf16x8, a), __builtin_bit_cast(bf16x8, b), c, 0, 0, 0); }
DI unsigned umax(unsigned a, unsigned b) { return a > b ? a : b; }
DI unsigned nosentf(float f) { const unsigned u = __float_as_uint(f); return u < 0xFFFFFFFEu ? u : 0xFFFFFFFEu; }
DI unsigned mx4(v4u v) { return umax(umax(v.x, v.y), umax(v.z, v.w)); }
DI bool none(bool p) { return __builtin_amdgcn_ballot_w64(p) == 0ull; }
#define CBAR() asm volatile("" ::: "memory")
#define LGKM0() asm volatile("s_waitcnt lgkmcnt(0)" ::: "memory")
#define SB() __builtin_amdgcn_sched_barrier(0)
#define PIN(x) asm volatile("" : "+v"(x))
#define F4(v) __builtin_bit_cast(f32x4, v)
#define U2F(u) __uint_as_float(u)
#define F2U(f) __float_as_uint(f)
template <int CTRL> DI float dpp(float x) { return __builtin_bit_cast(float, __builtin_amdgcn_mov_dpp(__builtin_bit_cast(int, x), CTRL, 0xf, 0xf, true)); }
constexpr int XOR1 = 0xB1, XOR2 = 0x4E, XOR7 = 0x141, ROR8 = 0x128, ROR4 = 0x124, BCAST = 0x150;
DI float sw16(float a, float b) { auto s = __builtin_amdgcn_permlane16_swap(F2U(a), F2U(b), false, false); return U2F(s[0]) + U2F(s[1]); }
DI float sw32(float a, float b) { auto s = __builtin_amdgcn_permlane32_swap(F2U(a), F2U(b), false, false); return U2F(s[0]) + U2F(s[1]); }
DI float x16sum(float x) { return sw16(x, x); }
DI float x32sum(float x) { return sw32(x, x); }
DI float x16max(float x) { auto s = __builtin_amdgcn_permlane16_swap(F2U(x), F2U(x), false, false); return fmaxf(U2F(s[0]), U2F(s[1])); }
DI float x32max(float x) { auto s = __builtin_amdgcn_permlane32_swap(F2U(x), F2U(x), false, false); return fmaxf(U2F(s[0]), U2F(s[1])); }
DI float rsum(float x) { x += dpp<XOR1>(x); x += dpp<XOR2>(x); x += dpp<XOR7>(x); x += dpp<ROR8>(x); return x; }
DI float wsum(float x) { return x32sum(x16sum(rsum(x))); }
DI float wmax(float x) { x = fmaxf(x, dpp<XOR1>(x)); x = fmaxf(x, dpp<XOR2>(x)); x = fmaxf(x, dpp<XOR7>(x)); x = fmaxf(x, dpp<ROR8>(x)); return x32max(x16max(x)); }
typedef __amdgpu_buffer_rsrc_t rsrc_t;
DI rsrc_t mkrs(const void* p, int bytes) { return __builtin_amdgcn_make_buffer_rsrc((void*)p, 0, bytes, 0x00020000); }
#define LD16(r, vo, so, aux) __builtin_amdgcn_raw_buffer_load_b128((r), (vo), (so), (aux))
#define LD8(r, vo, so, aux) __builtin_amdgcn_raw_buffer_load_b64((r), (vo), (so), (aux))
#define LD4(r, vo, so, aux) __builtin_amdgcn_raw_buffer_load_b32((r), (vo), (so), (aux))
#define ST16(v, r, vo, so, aux) __builtin_amdgcn_raw_buffer_store_b128((v), (r), (vo), (so), (aux))
#define ST8(v, r, vo, so, aux) __builtin_amdgcn_raw_buffer_store_b64((v), (r), (vo), (so), (aux))
#define ST4(v, r, vo, so, aux) __builtin_amdgcn_raw_buffer_store_b32((v), (r), (vo), (so), (aux))
constexpr int NT = 2, SC1 = 16;
DI void lds_wait(volatile LAS unsigned* f, unsigned want) {
#pragma nounroll
  for (int i = 0; i < SPIN * 8; ++i) { if (__builtin_amdgcn_readfirstlane(*f) >= want) break; __builtin_amdgcn_s_sleep(1); }
  CBAR();
}
DI void lds_arrive(LAS unsigned* c, int lane) {
  LGKM0();
  if (lane == 0) __hip_atomic_fetch_add(c, 1u, __ATOMIC_RELAXED, __HIP_MEMORY_SCOPE_WORKGROUP);
  CBAR();
}
#define BARRIER() do { LGKM0(); __builtin_amdgcn_s_barrier(); CBAR(); } while (0)
#define VMW(n) asm volatile("s_waitcnt vmcnt(%0)" :: "n"(n) : "memory")
DI v4u selfrag(unsigned p, const v4u& mk) { return v4u{p & mk.x, p & mk.y, p & mk.z, p & mk.w}; }
DI v4u selmask(int c) { const int r7 = c & 7; const unsigned hm = (r7 & 1) ? 0xffff0000u : 0x0000ffffu;
  return v4u{(r7 >> 1) == 0 ? hm : 0u, (r7 >> 1) == 1 ? hm : 0u, (r7 >> 1) == 2 ? hm : 0u, (r7 >> 1) == 3 ? hm : 0u}; }
DI unsigned permlo(unsigned a, unsigned b) { return __builtin_amdgcn_perm(b, a, 0x05040100u); }
DI unsigned permhi(unsigned a, unsigned b) { return __builtin_amdgcn_perm(b, a, 0x07060302u); }
template <int N> DI unsigned badtags(const v4u (&u)[N], unsigned elo, unsigned ehi) {
  unsigned bad = 0;
#pragma unroll
  for (int k = 0; k < N; ++k) bad |= (u[k].y ^ elo) | (u[k].w ^ ehi);
  return bad;
}

constexpr int D = 640, HQ = 32, SK = 16384, DH = 128, WIN = 4096, DFF = 1728;
#define SV_Q1 2
#define STAMP 0
constexpr int WS_EPOCH = 0;
constexpr int WS_MP = 4096;
constexpr int WS_QF = WS_MP + 61440;
constexpr int WS_OP = WS_QF + 16384;
constexpr int WS_YP = WS_OP + 122880;
constexpr int WS_FP = WS_YP + 20480;
constexpr int WS_BYTES = WS_FP + 69120;
constexpr int WS_STAMP = 1 << 20;
constexpr int L_K = 0;
constexpr int L_P = 147456;
constexpr int L_Q = L_P + 6656;
constexpr int L_OM = L_Q + 256;
constexpr int L_F = L_OM + 4096;
constexpr int L_END = L_F + 256;
enum { F_KA = 0  , F_KB = 4  , F_FREE = 8  , F_QRDY = 12, F_CML = 13, F_M = 16  , F_L = 20   };
constexpr int S_W1 = 0;
constexpr int S_MB = 81920;
constexpr int S_S = S_MB + 12288;
constexpr int S_HT = S_S + 1280;
constexpr int S_Y = S_HT + 2560;
constexpr int S_Z = S_Y + 4096;
constexpr int S_X1 = S_Z + 2560;
constexpr int S_H = S_X1 + 3072;
constexpr int S_ST = S_H + 128;
static_assert(S_ST + 64 <= L_END, "");

#define TS(k) do {} while (0)
#define TS_FLUSH() do {} while (0)
#define DMA(m0_, vo_, srd_, so_) do { unsigned keep_; \
    asm volatile("s_mov_b32 %0, m0\n\ts_mov_b32 m0, %1\n\ts_nop 0\n\tbuffer_load_dwordx4 %2, %3, %4 offen nt lds\n\ts_mov_b32 m0, %0" \
                 : "=&s"(keep_) : "s"(m0_), "v"(vo_), "s"(srd_), "s"(so_) : "memory"); } while (0)

__global__ void __launch_bounds__(512, 2) fused_kernel(const u16* kc, const u16* vc, char* ws, const u16* wmod, const u16* cvec, const u16* x,
    const u16* wq, const u16* wo, const u16* w1, const u16* w2, u16* out) {
  __shared__ __attribute__((aligned(16))) char smem[L_END];
  LAS char* L = (LAS char*)smem;
  const int tid = threadIdx.x, lane = tid & 63, wave = __builtin_amdgcn_readfirstlane(tid >> 6);
  const int b = blockIdx.x, xg = b & 7, r = b >> 3;
  const int g = lane >> 4, c = lane & 15;
  TS(0);
  const unsigned lds0 = (unsigned)(uintptr_t)L;

  if (__builtin_expect(r < 24, 1)) {
    volatile LAS unsigned* FL = (volatile LAS unsigned*)(L + L_F);
    const int hd = xg * 4 + (r & 3), sp = r >> 2, j = wave & 3;
    const int nr = sp < 4 ? 43 : 42, r0 = sp * 43 - (sp == 5 ? 1 : 0);
    const size_t kvoff = ((size_t)hd * SK + (SK - WIN) + r0 * 16) * DH;
    if (__builtin_expect(wave < 4, 1)) {
      const unsigned long long ka = (unsigned long long)(kc + kvoff);
      const v4u srdK = {(unsigned)ka, (unsigned)(ka >> 32), (unsigned)nr * 4096u, 0x00020000u};
      int vo[4];
#pragma unroll
      for (int q = 0; q < 4; ++q) vo[q] = g * 256 + ((c ^ (4 * q + g)) * 16);
      const unsigned lbase = lds0 + L_K + j * 36864;
      asm volatile("s_nop 4" ::: "memory");
#pragma nounroll
      for (int i4 = 0; i4 < 11; ++i4) {
        if (i4 == 9) lds_wait(FL + F_FREE + j, 1);
        const unsigned mb = lbase + (i4 < 9 ? i4 : i4 - 9) * 4096, sb = i4 * 16384 + j * 1024;
#pragma unroll
        for (int q = 0; q < 4; ++q) { DMA(mb + q * 1024, vo[q], srdK, sb + q * 4096); if (i4 < KV_N1) VMW(KV_W1); else VMW(KV_WIN); }
        if (i4 == 1) __builtin_amdgcn_s_barrier();
        if (i4 == (16 + KV_WIN + 3) / 4 - 1) FL[F_KA + j] = 1u;
      }
      TS(1);
      const rsrc_t rws = mkrs(ws, WS_BYTES);
      const rsrc_t rv = mkrs(vc + kvoff, nr * 4096);
      const v2u ep = LD8(rws, 0, WS_EPOCH, SC1);
      v4u V[43];
#pragma unroll
      for (int i = 0; i < 43; ++i) {
        V[i] = LD16(rv, lane * 16, i * 4096 + j * 1024, NT);
        VMW(KV_WIN);
        if (i == KV_WIN - 1) { FL[F_KB + j] = 1u; TS(2); }
        SB();
      }
      TS(3);
      lds_wait(FL + F_CML, 4);
      TS(4);
      const unsigned long long e1 = (((unsigned long long)ep.y << 32) | ep.x) + 1ull;
      unsigned elo = (unsigned)e1, ehi = (unsigned)(e1 >> 32);
      const v4u mk = selmask(c);
      const float m0_ = U2F(FL[F_M]), m1_ = U2F(FL[F_M + 1]), m2_ = U2F(FL[F_M + 2]), m3_ = U2F(FL[F_M + 3]);
      float mcu = fmaxf(fmaxf(m0_, m1_), fmaxf(m2_, m3_));
      float lcu = U2F(FL[F_L]) * __expf(m0_ - mcu) + U2F(FL[F_L + 1]) * __expf(m1_ - mcu) + U2F(FL[F_L + 2]) * __expf(m2_ - mcu) + U2F(FL[F_L + 3]) * __expf(m3_ - mcu);
      float fj = __expf(U2F(FL[F_M + j]) - mcu) / lcu;
      int sto = lane < 32 ? c * 32 + g * 16 : (lane == 32 ? 512 : OOB);
      PIN(fj); PIN(mcu); PIN(lcu); PIN(sto); PIN(elo); PIN(ehi);
      const LAS v4u* pt = (const LAS v4u*)(L + L_P + (((j * 2 + (c >> 3)) * 4 + g) * 52) * 4);
      v4u p4[11];
#pragma unroll
      for (int i4 = 0; i4 < 11; ++i4) p4[i4] = pt[i4];
      SB();
      f32x4 acc = {0.f, 0.f, 0.f, 0.f};
#pragma unroll
      for (int i = 0; i < 43; ++i) acc = mfma(selfrag(p4[i >> 2][i & 3], mk), V[i], acc);
      ((LAS f32x4*)(L + L_OM))[j * 64 + lane] = acc * fj;
      TS(5);
      BARRIER();
      if (j == 0) {
        const LAS f32x4* om = (const LAS f32x4*)(L + L_OM) + (lane & 31);
        const f32x4 o = ((om[0] + om[32]) + (om[64] + om[96])) + ((om[128] + om[160]) + (om[192] + om[224]));
        const bool ml = lane >= 32;
        const v4u st = {ml ? F2U(mcu) : cvtpk(o.x, o.y), elo, ml ? F2U(lcu) : cvtpk(o.z, o.w), ehi};
        ST16(st, rws, sto, WS_OP + (hd * 6 + sp) * 640, SC1);
        TS(6);
      }
      TS_FLUSH();
      return;
    }
    FL[lane] = 0u;
    BARRIER();
    const LAS char* kb = L + L_K + j * 36864 + c * 256;
    v4u kf[11][4];
    lds_wait(FL + F_KA + j, 1);
#pragma unroll
    for (int t = 0; t < 4; ++t)
#pragma unroll
      for (int ks = 0; ks < 4; ++ks) kf[t][ks] = *(const LAS v4u*)(kb + t * 4096 + (((4 * ks + g) ^ c) * 16));
    SB();
    LGKM0();
    FL[F_FREE + j] = 1u;
    TS(1);
    if (j == 0) {
      const rsrc_t rws = mkrs(ws, WS_BYTES);
      const v2u ep = LD8(rws, 0, WS_EPOCH, SC1);
      const unsigned long long e1 = (((unsigned long long)ep.y << 32) | ep.x) + 1ull;
      const unsigned elo = (unsigned)e1, ehi = (unsigned)(e1 >> 32);
      v4u qu[1];
#pragma nounroll
      for (int it = 0; it < SPIN; ++it) {
        CBAR();
        qu[0] = LD16(rws, (lane & 31) * 16, WS_QF + hd * 512, SC1);
        if (none(badtags<1>(qu, elo, ehi) != 0u)) break;
      }
      ((LAS v2u*)(L + L_Q))[lane & 31] = v2u{qu[0].x, qu[0].z};
      LGKM0();
      FL[F_QRDY] = 1u;
    }
    TS(2);
    lds_wait(FL + F_KB + j, 1);
#pragma unroll
    for (int t = 4; t < 11; ++t)
#pragma unroll
      for (int ks = 0; ks < 4; ++ks) kf[t][ks] = *(const LAS v4u*)(kb + (t < 9 ? t : t - 9) * 4096 + (((4 * ks + g) ^ c) * 16));
    SB();
    lds_wait(FL + F_QRDY, 1);
    TS(3);
    v4u qa[4];
#pragma unroll
    for (int ks = 0; ks < 4; ++ks) qa[ks] = *(const LAS v4u*)(L + L_Q + (4 * ks + g) * 16);
    float sc[11];
#pragma unroll
    for (int t = 0; t < 11; ++t) {
      f32x4 d = {0.f, 0.f, 0.f, 0.f};
#pragma unroll
      for (int ks = 0; ks < 4; ++ks) d = mfma(qa[ks], kf[t][ks], d);
      sc[t] = d.x;
    }
#pragma unroll
    for (int t = 0; t < 11; ++t) PIN(sc[t]);
    const bool g1 = g & 1, g2 = g & 2;
    float s0 = g2 ? (g1 ? sc[3] : sc[2]) : (g1 ? sc[1] : sc[0]);
    float s1 = g2 ? (g1 ? sc[7] : sc[6]) : (g1 ? sc[5] : sc[4]);
    float s2 = g2 ? sc[10] : (g1 ? sc[9] : sc[8]);
    s0 = rbf(rbf(s0) * 0.08838834764831845f); s1 = rbf(rbf(s1) * 0.08838834764831845f); s2 = rbf(rbf(s2) * 0.08838834764831845f);
    const bool v2 = 128 + lane < 4 * nr;
    if (!v2) s2 = -INFINITY;
    const float m = wmax(fmaxf(fmaxf(s0, s1), s2));
    const float p0 = __expf(s0 - m), p1 = __expf(s1 - m), p2 = v2 ? __expf(s2 - m) : 0.f;
    const float l = wsum((p0 + p1) + p2);
    const unsigned h0 = cvtpk(p0, p0), h1 = cvtpk(p1, p1), h2 = cvtpk(p2, p2);
    const float q0 = p0 - bflo(h0), q1 = p1 - bflo(h1), q2 = p2 - bflo(h2);
    LAS unsigned* pw = (LAS unsigned*)(L + L_P) + (j * 8 + (lane & 3)) * 52 + (lane >> 2);
    pw[0] = h0; pw[16] = h1; pw[32] = h2; pw[208] = cvtpk(q0, q0); pw[224] = cvtpk(q1, q1); pw[240] = cvtpk(q2, q2);
    if (lane == 0) { FL[F_M + j] = F2U(m); FL[F_L + j] = F2U(l); }
    lds_arrive((LAS unsigned*)(L + L_F) + F_CML, lane);
    TS(4);
    TS_FLUSH();
    return;
  }

  const int w = wave, k = r - 24, sid = xg * 8 + k;
  const bool role_b = k < 5, role_c = k >= 5 || (k == 0 && xg < 3), role_m = !(k == 0 && xg < 4);
  const int uc = k >= 5 ? xg * 3 + k - 5 : 24 + xg;
  const int mi = xg < 4 ? xg * 7 + k - 1 : 28 + (xg - 4) * 8 + k;
  const int chi = c & 7, rho = c >> 3, kh = mi & 1;
  const rsrc_t rws = mkrs(ws, WS_BYTES);
  const rsrc_t rm = mkrs(wmod, role_m ? D * 6 * D * 2 : 0);
  const rsrc_t rx = mkrs(x, D * 2);
  const rsrc_t rq = mkrs(wq, D * HQ * DH * 2);
  const int mvo = (5 * (2 * g + rho)) * 7680 + chi * 16, mso = (320 * kh + 40 * w) * 7680 + (mi >> 1) * 128;
  const int qvo = (10 * (2 * g + rho)) * 8192 + chi * 16, qso = (80 * w) * 8192 + sid * 128;
  v4u WM[5], WQ[10];
  const v2u cv = LD8(mkrs(cvec, D * 2), (lane < 10 ? lane : 0) * 8, kh * 640 + w * 80, 0);
  v2u ep = LD8(rws, 0, WS_EPOCH, SC1);
#pragma unroll
  for (int i = 0; i < 5; ++i) WM[i] = LD16(rm, mvo, mso + i * 7680, NT);
  SB(); CBAR();
  __builtin_amdgcn_s_barrier();
  CBAR();
#pragma unroll
  for (int i = 0; i < SV_Q1; ++i) WQ[i] = LD16(rq, qvo, qso + i * 8192, NT);
  SB(); CBAR();
  const v4u mk = selmask(c);
  unsigned sv5[5];
  {
    float cf[4] = {bflo(cv.x), bfhi(cv.x), bflo(cv.y), bfhi(cv.y)};
    unsigned sd[4];
#pragma unroll
    for (int i = 0; i < 4; ++i) { const float sv = cf[i] / (1.f + __expf(-cf[i])); sd[i] = cvtpk(sv, sv); }
    if (lane < 10) ((LAS v4u*)(L + S_S))[w * 10 + lane] = v4u{sd[0], sd[1], sd[2], sd[3]};
    LGKM0();
#pragma unroll
    for (int i = 0; i < 5; ++i) sv5[i] = ((const LAS unsigned*)(L + S_S))[w * 40 + 5 * (2 * g + rho) + i];
  }
  PIN(ep);
  const unsigned long long e1 = (((unsigned long long)ep.y << 32) | ep.x) + 1ull;
  const unsigned elo = (unsigned)e1, ehi = (unsigned)(e1 >> 32);
  const int mbw = ((w * 2 + rho) * 16 + chi * 2 + (g & 1)) * 16;
  auto merge16 = [&](int base) {
    const LAS f32x2* mb = (const LAS f32x2*)(L + base) + (lane & 31);
    f32x2 t[16];
#pragma unroll
    for (int i = 0; i < 16; ++i) t[i] = mb[32 * i];
    return (((t[0] + t[1]) + (t[2] + t[3])) + ((t[4] + t[5]) + (t[6] + t[7]))) + (((t[8] + t[9]) + (t[10] + t[11])) + ((t[12] + t[13]) + (t[14] + t[15])));
  };
  auto mod_unit = [&](int base, int um) {
    f32x4 acc = {0.f, 0.f, 0.f, 0.f};
#pragma unroll
    for (int i = 0; i < 5; ++i) acc = mfma(selfrag(sv5[i], mk), WM[i], acc);
    if ((g >> 1) == rho) *(LAS f32x4*)(L + base + mbw) = acc;
    BARRIER();
    if (w == 0) { const f32x2 mv = merge16(base); ST16((v4u{F2U(mv.x), elo, F2U(mv.y), ehi}), rws, (lane < 32 && role_m) ? lane * 16 : OOB, WS_MP + kh * 30720 + (um >> 1) * 512, SC1); }
  };
  mod_unit(S_MB, mi);
  TS(1);
  unsigned xr[5];
#pragma unroll
  for (int i = 0; i < 5; ++i) xr[i] = LD4(rx, lane * 4, i * 256, 0);
  const unsigned xme = LD4(rx, (lane < 40 ? lane : 0) * 4, w * 160, 0);
  ST16((v4u{SENT, SENT, SENT, SENT}), rws, w == 1 ? ((role_b && lane < 32) ? WS_YP + xg * 2560 + k * 512 + lane * 16 : OOB)
                                          : ((role_c && w >= 2 && (w - 2) * 64 + lane < 160) ? WS_FP + uc * 2560 + (w - 2) * 1024 + lane * 16 : OOB), 0, SC1);
#pragma unroll
  for (int i = SV_Q1; i < 10; ++i) WQ[i] = LD16(rq, qvo, qso + i * 8192, NT);
  SB(); CBAR();
  {
    float s = 0.f;
#pragma unroll
    for (int i = 0; i < 5; ++i) s += bflo(xr[i]) + bfhi(xr[i]);
    float mean = wsum(s) * (1.f / D);
    float v = 0.f;
#pragma unroll
    for (int i = 0; i < 5; ++i) { const float d0 = bflo(xr[i]) - mean, d1 = bfhi(xr[i]) - mean; v += d0 * d0 + d1 * d1; }
    v = wsum(v) * (1.f / D);
    const float den = rbf(sqrtf(rbf(rbf(v) + 1e-5f)));
    mean = rbf(mean);
    float n0 = rbf(rbf(bflo(xme) - mean) / den), n1 = rbf(rbf(bfhi(xme) - mean) / den);
    PIN(n0); PIN(n1);
    const int vo_ = (lane < 40 ? lane : 0) * 16 + w * 640;
    v4u u[4];
    u[0] = LD16(rws, vo_, WS_MP, SC1); u[1] = LD16(rws, vo_, WS_MP + 30720, SC1); u[2] = LD16(rws, vo_, WS_MP + 5120, SC1); u[3] = LD16(rws, vo_, WS_MP + 35840, SC1);
#pragma unroll
    for (int i = 0; i < 5; ++i) WM[i] = LD16(rm, mvo, mso + 30 * 128 + i * 7680, NT);
    SB(); CBAR();
    if (!none(badtags<4>(u, elo, ehi) != 0u)) {
#pragma nounroll
      for (int it = 0; it < SPIN; ++it) {
        CBAR();
        u[0] = LD16(rws, vo_, WS_MP, SC1); u[1] = LD16(rws, vo_, WS_MP + 30720, SC1); u[2] = LD16(rws, vo_, WS_MP + 5120, SC1); u[3] = LD16(rws, vo_, WS_MP + 35840, SC1);
        if (none(badtags<4>(u, elo, ehi) != 0u)) break;
      }
    }
    TS(2);
    const float sa0 = rbf(U2F(u[0].x) + U2F(u[1].x)), sa1 = rbf(U2F(u[0].z) + U2F(u[1].z)), ca0 = rbf(U2F(u[2].x) + U2F(u[3].x)), ca1 = rbf(U2F(u[2].z) + U2F(u[3].z));
    const float h0 = rbf(n0 * rbf(1.f + ca0)) + sa0, h1 = rbf(n1 * rbf(1.f + ca1)) + sa1;
    if (lane < 40) ((LAS v2u*)(L + S_HT))[w * 40 + lane] = v2u{cvtpk(h0, h0), cvtpk(h1, h1)};
    LGKM0();
    const LAS v2u* ht = (const LAS v2u*)(L + S_HT) + w * 40 + 5 * (2 * g + rho);
    f32x4 acc = {0.f, 0.f, 0.f, 0.f};
#pragma unroll
    for (int i = 0; i < 5; ++i) { const v2u h2 = ht[i]; acc = mfma(selfrag(h2.x, mk), WQ[2 * i], acc); acc = mfma(selfrag(h2.y, mk), WQ[2 * i + 1], acc); }
    if ((g >> 1) == rho) *(LAS f32x4*)(L + S_MB + 4096 + mbw) = acc;
    BARRIER();
    if (w == 0) {
      const f32x2 qv = merge16(S_MB + 4096);
      const unsigned qp = cvtpk(qv.x, qv.y), qn = F2U(dpp<XOR1>(U2F(qp)));
      ST16((v4u{qp, elo, qn, ehi}), rws, (lane < 32 && !(lane & 1)) ? lane * 8 : OOB, WS_QF + sid * 256, SC1);
    }
  }
  TS(3);
  const int hb = xg * 4 + (w >> 1), kb = w & 1;
  v4u BF[2][8];
  v4u B2[2][2][4];
  v2u W2r[2][16];
  const rsrc_t r2 = mkrs(w2, DFF * D * 2);
  auto issue_w2 = [&]() {
#pragma unroll
    for (int i = 0; i < 16; ++i) W2r[0][i] = LD8(r2, (8 * g) * 1280 + c * 8, (64 * uc + (i >> 3) * 32 + (i & 7)) * 1280 + w * 128, NT);
    if (w < 2) {
#pragma unroll
      for (int i = 0; i < 16; ++i) W2r[1][i] = LD8(r2, (8 * g) * 1280 + c * 8, (64 * uc + (i >> 3) * 32 + (i & 7)) * 1280 + (8 + w) * 128, NT);
    }
    SB(); CBAR();
  };
  auto issue_w1 = [&](int i0, int i1) {
    const unsigned long long wa = (unsigned long long)w1;
    const v4u srd1 = {(unsigned)wa, (unsigned)(wa >> 32), (unsigned)(D * DFF * 2), 0x00020000u};
    const int f0 = (g & 1) | ((w >> 2) << 2);
    const int voa = (lane >> 3) * 3456 + (((lane & 7) ^ f0) * 16), vob = (lane >> 3) * 3456 + (((lane & 7) ^ f0 ^ 2) * 16);
    asm volatile("s_nop 4" ::: "memory");
#pragma nounroll
    for (int i = i0; i < i1; i += 2) {
      const unsigned m0v = lds0 + S_W1 + (w * 10 + i) * 1024, so = (w * 80 + i * 8) * 3456 + uc * 128;
      DMA(m0v, voa, srd1, so); DMA(m0v + 1024, vob, srd1, so + 8 * 3456);
    }
  };
#pragma unroll
  for (int i = 0; i < 5; ++i) PIN(WM[i]);
  if (role_b) {
    v4u WB[16];
    const rsrc_t ro = mkrs(wo, HQ * DH * D * 2);
#define LDWB(i) WB[i] = LD16(ro, (4 * g) * 1280 + c * 16, (hb * 128 + 64 * kb + 32 * (((i) >> 2) & 1) + 16 * ((i) >> 3) + ((i) & 3)) * 1280 + k * 256, NT)
#pragma unroll
    for (int i = 0; i < 3; ++i) LDWB(i);
    SB(); CBAR();
    mod_unit(S_MB + 8192, 60 + mi);
    TS(4);
#pragma unroll
    for (int i = 3; i < 16; ++i) LDWB(i);
    SB(); CBAR();
    if (role_c) issue_w1(0, 10);
#pragma unroll
    for (int hf = 0; hf < 2; ++hf) {
#pragma unroll
      for (int m = 0; m < 4; ++m)
#pragma unroll
        for (int d = 0; d < 4; ++d) { BF[hf][2 * d][m] = permlo(WB[hf * 8 + 2 * m][d], WB[hf * 8 + 2 * m + 1][d]); BF[hf][2 * d + 1][m] = permhi(WB[hf * 8 + 2 * m][d], WB[hf * 8 + 2 * m + 1][d]); }
#pragma unroll
      for (int t = 0; t < 8; ++t) PIN(BF[hf][t]);
    }
  } else {
    issue_w1(0, 4);
    mod_unit(S_MB + 8192, 60 + mi);
    TS(4);
    issue_w1(4, 10);
  }
  v4u B1[10];
  f32x2 zg = {0.f, 0.f}, zx = {0.f, 0.f}, ga2 = {0.f, 0.f}, x2 = {0.f, 0.f};
  float Cc = 0.f, Ss = 0.f;
  const int eo = (lane < 40 ? lane : 0) * 8 + w * 320;
  const LAS char* zr = L + S_Z + (c & 1) * 640 + g * 16;
  if (role_c) {
    v4u mm[7];
#pragma nounroll
    for (int it = 0; it < SPIN; ++it) {
      CBAR();
#pragma unroll
      for (int i = 0; i < 6; ++i) mm[i] = LD16(rws, eo * 2, WS_MP + (i & 1) * 30720 + (2 + (i >> 1)) * 5120, SC1);
      mm[6] = LD16(rws, (lane & 7) * 256, WS_QF + w * 2048, SC1);
      if (none(badtags<7>(mm, elo, ehi) != 0u)) break;
      __builtin_amdgcn_s_sleep(8);
    }
    ga2 = f32x2{rbf(U2F(mm[0].x) + U2F(mm[1].x)), rbf(U2F(mm[0].z) + U2F(mm[1].z))};
    const f32x2 c1 = {rbf(1.f + rbf(U2F(mm[4].x) + U2F(mm[5].x))), rbf(1.f + rbf(U2F(mm[4].z) + U2F(mm[5].z)))};
    x2 = f32x2{bflo(xme), bfhi(xme)};
    zg = ga2 * c1; zx = x2 * c1;
    if (lane < 40) { *(LAS unsigned*)(L + S_Z + (eo >> 1)) = cvtpk(c1.x, c1.y); *(LAS unsigned*)(L + S_Z + 1280 + (eo >> 1)) = cvtpk(U2F(mm[2].x) + U2F(mm[3].x), U2F(mm[2].z) + U2F(mm[3].z)); }
    if (w == 0) *(LAS f32x2*)(L + S_X1 + 2560 + lane * 8) = f32x2{0.f, 0.f};
    issue_w2();
    BARRIER();
    {
      typedef short v4s __attribute__((ext_vector_type(4)));
      const int q_ = c >> 2, p_ = c & 3, f_ = (q_ >> 1) | ((g & 1) << 1) | ((p_ >> 1) << 2);
      LAS char* wi = L + S_W1 + (320 * (p_ >> 1) + 8 * g + q_) * 128 + ((w ^ f_) * 16) + (p_ & 1) * 8;
#pragma unroll
      for (int ks = 0; ks < 10; ++ks) {
        const v2u lo = __builtin_bit_cast(v2u, __builtin_amdgcn_ds_read_tr16_b64_v4i16((LAS v4s*)(wi + ks * 4096)));
        const v2u hi = __builtin_bit_cast(v2u, __builtin_amdgcn_ds_read_tr16_b64_v4i16((LAS v4s*)(wi + ks * 4096 + 512)));
        B1[ks] = v4u{lo.x, lo.y, hi.x, hi.y};
      }
#pragma unroll
      for (int ks = 0; ks < 10; ++ks) PIN(B1[ks]);
    }
    const LAS char* zr2 = zr + ((c >> 1) & 1) * 1280;
    f32x4 d0 = {0.f, 0.f, 0.f, 0.f};
#pragma unroll
    for (int ks = 0; ks < 10; ++ks) d0 = mfma(*(const LAS v4u*)(zr2 + ks * 64), B1[ks], d0);
    Cc = d0.x + dpp<ROR8>(d0.y); Ss = d0.z + dpp<ROR8>(d0.w);
#pragma unroll
    for (int tk = 0; tk < 2; ++tk)
#pragma unroll
      for (int ks = 0; ks < 2; ++ks) {
#pragma unroll
        for (int m = 0; m < 4; ++m) {
          const v2u e = W2r[tk][ks * 8 + 2 * m], o = W2r[tk][ks * 8 + 2 * m + 1];
          B2[tk][ks][0][m] = permlo(e.x, o.x); B2[tk][ks][1][m] = permhi(e.x, o.x); B2[tk][ks][2][m] = permlo(e.y, o.y); B2[tk][ks][3][m] = permhi(e.y, o.y);
        }
#pragma unroll
        for (int t = 0; t < 4; ++t) PIN(B2[tk][ks][t]);
      }
    PIN(Cc); PIN(Ss); PIN(zg); PIN(zx); PIN(ga2); PIN(x2);
    BARRIER();
  }
  const bool role_d = role_b && ((w == 1 && k > 0) || (w == 2 && k == 1));
  const int v = xg * 5 + (w == 1 ? k : 0);
  const int q4 = lane & 3, part = lane >> 2;
  float dx = 0.f, dga = 0.f, dgb = 0.f;
  if (role_d) {
    const unsigned xv = LD4(rx, q4 * 8 + (g >> 1) * 4, v * 32, 0);
    v4u mm[5];
#pragma nounroll
    for (int it = 0; it < SPIN; ++it) {
      CBAR();
#pragma unroll
      for (int i = 0; i < 4; ++i) mm[i] = LD16(rws, q4 * 32 + (g >> 1) * 16, WS_MP + (i & 1) * 30720 + (2 + 3 * (i >> 1)) * 5120 + v * 128, SC1);
      mm[4] = LD16(rws, lane * 256, WS_QF, SC1);
      if (none(badtags<5>(mm, elo, ehi) != 0u)) break;
      __builtin_amdgcn_s_sleep(8);
    }
    const bool odd = g & 1;
    dx = odd ? bfhi(xv) : bflo(xv);
    dga = rbf(odd ? U2F(mm[0].z) + U2F(mm[1].z) : U2F(mm[0].x) + U2F(mm[1].x));
    dgb = rbf(odd ? U2F(mm[2].z) + U2F(mm[3].z) : U2F(mm[2].x) + U2F(mm[3].x));
    PIN(dx); PIN(dga); PIN(dgb);
  }
  TS(5);
  if (role_b) {
    const int s1 = (c & 7) < 6 ? (c & 7) : 0, s2 = (c + 4 * g) & 7;
    int vo1 = s1 * 640 + kb * 256 + (c >> 3) * 64 + g * 16, vo2 = (s2 < 6 ? s2 : 0) * 640 + 512;
    unsigned m_lo = c < 8 ? ~0u : 0u, m_hi = ~m_lo;
    float live = s2 < 6 ? 1.f : 0.f;
    int lw = S_Y + (w * 128 + c * 8 + g) * 4;
    PIN(vo1); PIN(vo2); PIN(lw); PIN(m_lo); PIN(m_hi); PIN(live);
    v4u oa[3];
#pragma nounroll
    for (int it = 0; it < SPIN; ++it) {
      CBAR();
      oa[2] = LD16(rws, vo2, WS_OP + hb * 3840, SC1);
      oa[0] = LD16(rws, vo1, WS_OP + hb * 3840, SC1);
      oa[1] = LD16(rws, vo1, WS_OP + hb * 3840 + 128, SC1);
      if (none(badtags<3>(oa, elo, ehi) != 0u)) break;
    }
    TS(6);
    const v4u a0 = {oa[0].x & m_lo, oa[0].z & m_lo, oa[1].x & m_lo, oa[1].z & m_lo}, a1 = {oa[0].x & m_hi, oa[0].z & m_hi, oa[1].x & m_hi, oa[1].z & m_hi};
    f32x4 d[8];
#pragma unroll
    for (int t = 0; t < 8; ++t) d[t] = mfma(a0, BF[0][t], f32x4{0.f, 0.f, 0.f, 0.f});
#pragma unroll
    for (int t = 0; t < 8; ++t) d[t] = mfma(a1, BF[1][t], d[t]);
    const float ms = U2F(oa[2].x);
    float M = fmaxf(ms, dpp<XOR1>(ms)); M = fmaxf(M, dpp<XOR2>(M)); M = fmaxf(M, dpp<XOR7>(M));
    const float e = U2F(oa[2].z) * __expf(ms - M) * live;
    float Ls = e + dpp<XOR1>(e); Ls += dpp<XOR2>(Ls); Ls += dpp<XOR7>(Ls);
    const float wt = e * __builtin_amdgcn_rcpf(Ls);
    const float w0 = dpp<BCAST + 0>(wt), w1_ = dpp<BCAST + 1>(wt), w2_ = dpp<BCAST + 2>(wt), w3 = dpp<BCAST + 3>(wt);
    float y[8];
#pragma unroll
    for (int t = 0; t < 8; ++t) y[t] = (d[t].x * w0 + d[t].y * w1_) + (d[t].z * w2_ + d[t].w * w3);
    const float ya = sw32(sw16(y[0], y[1]), sw16(y[2], y[3])), yb = sw32(sw16(y[4], y[5]), sw16(y[6], y[7]));
    *(LAS float*)(L + lw) = ya; *(LAS float*)(L + lw + 16) = yb;
    BARRIER();
    if (w == 0) {
      const LAS f32x2* tp = (const LAS f32x2*)(L + S_Y) + lane;
      const f32x2 yy = ((tp[0] + tp[64]) + (tp[128] + tp[192])) + ((tp[256] + tp[320]) + (tp[384] + tp[448]));
      ST8((v2u{nosentf(yy.x), nosentf(yy.y)}), rws, lane * 8, WS_YP + xg * 2560 + k * 512, SC1);
    }
    TS(7);
  }
  if (role_c) {
    int zw = lane < 40 ? S_Z + (eo >> 1) : S_ST, xw = lane < 40 ? S_X1 + eo : S_ST + 8;
    int hw = lane < 8 ? S_H + (w * 8 + lane) * 2 : S_ST + 16;
    int sto = lane < 16 ? lane * 16 : OOB;
    PIN(zw); PIN(xw); PIN(hw); PIN(sto);
    f32x2 at;
#pragma nounroll
    for (int it = 0; it < SPIN; ++it) {
      CBAR();
      v2u yp[8];
#pragma unroll
      for (int i = 0; i < 8; ++i) yp[i] = LD8(rws, eo, WS_YP + i * 2560, SC1);
#define F2(v_) __builtin_bit_cast(f32x2, v_)
      at = ((F2(yp[0]) + F2(yp[1])) + (F2(yp[2]) + F2(yp[3]))) + ((F2(yp[4]) + F2(yp[5])) + (F2(yp[6]) + F2(yp[7])));
      if (__builtin_expect(none(__builtin_isunordered(at.x, at.y)), 1)) break;
      unsigned mx = 0;
#pragma unroll
      for (int i = 0; i < 8; ++i) mx = umax(mx, umax(yp[i].x, yp[i].y));
      if (none(mx == SENT)) break;
    }
    TS(8);
    const f32x2 z = zx + zg * at, x1 = x2 + ga2 * at;
    *(LAS unsigned*)(L + zw) = cvtpk(z.x, z.y);
    *(LAS f32x2*)(L + xw) = x1;
    BARRIER();
    v4u af[10];
#pragma unroll
    for (int ks = 0; ks < 10; ++ks) af[ks] = *(const LAS v4u*)(zr + ks * 64);
    const LAS f32x4* xp = (const LAS f32x4*)(L + S_X1) + lane;
    const f32x4 xa = xp[0], xb = xp[64], xc = xp[128];
    SB();
    f32x4 d0 = {0.f, 0.f, 0.f, 0.f}, d1 = {0.f, 0.f, 0.f, 0.f};
#pragma unroll
    for (int ks = 0; ks < 5; ++ks) { d0 = mfma(af[2 * ks], B1[2 * ks], d0); d1 = mfma(af[2 * ks + 1], B1[2 * ks + 1], d1); }
    const f32x4 t1 = (xa + xb) + xc, t2 = (xa * xa + xb * xb) + xc * xc;
    float s12 = sw16((t1.x + t1.y) + (t1.z + t1.w), (t2.x + t2.y) + (t2.z + t2.w));
    s12 = x32sum(rsum(s12));
    const float mean = U2F(__builtin_amdgcn_readlane(F2U(s12), 0)) * (1.f / D);
    const float var = fmaxf(U2F(__builtin_amdgcn_readlane(F2U(s12), 16)) * (1.f / D) - mean * mean, 0.f);
    const float rden = __builtin_amdgcn_rcpf(rbf(__builtin_amdgcn_sqrtf(rbf(rbf(var) + 1e-5f))));
    const float tt = Ss - Cc * (rbf(mean) * rden);
    const float uu = (d0.x + d1.x) + dpp<ROR8>(d0.y + d1.y);
    *(LAS __bf16*)(L + hw) = (__bf16)fmaxf(uu * rden + tt, 0.f);
    BARRIER();
    const v4u ha0 = *(const LAS v4u*)(L + S_H + g * 16), ha1 = *(const LAS v4u*)(L + S_H + 64 + g * 16);
    {
      f32x4 o[4];
#pragma unroll
      for (int t = 0; t < 4; ++t) o[t] = mfma(ha0, B2[0][0][t], f32x4{0.f, 0.f, 0.f, 0.f});
#pragma unroll
      for (int t = 0; t < 4; ++t) o[t] = mfma(ha1, B2[0][1][t], o[t]);
      ST16((v4u{nosentf(o[0].x), nosentf(o[1].x), nosentf(o[2].x), nosentf(o[3].x)}), rws, sto, WS_FP + uc * 2560 + w * 256, SC1);
    }
    if (w < 2) {
      f32x4 o[4];
#pragma unroll
      for (int t = 0; t < 4; ++t) o[t] = mfma(ha0, B2[1][0][t], f32x4{0.f, 0.f, 0.f, 0.f});
#pragma unroll
      for (int t = 0; t < 4; ++t) o[t] = mfma(ha1, B2[1][1][t], o[t]);
      ST16((v4u{nosentf(o[0].x), nosentf(o[1].x), nosentf(o[2].x), nosentf(o[3].x)}), rws, sto, WS_FP + uc * 2560 + (8 + w) * 256, SC1);
    }
    TS(9);
  }
  if (role_d) {
    v4u yq;
#pragma nounroll
    for (int it = 0; it < SPIN; ++it) {
      CBAR();
      yq = LD16(rws, q4 * 16 + (part & 7) * 2560, WS_YP + v * 64, SC1);
      if (none(mx4(yq) == SENT)) break;
    }
    auto red = [&](f32x4 t) { float s = sw32(sw16(t.x, t.y), sw16(t.z, t.w)); s += dpp<ROR4>(s); s += dpp<ROR8>(s); return s; };
    float x1 = rbf(dx + rbf(dga * rbf(0.5f * red(F4(yq)))));
    int o0 = q4 * 16 + part * 2560, o1 = q4 * 16 + (part < 11 ? part : 0) * 2560;
    float a1 = part < 11 ? 1.f : 0.f;
    PIN(x1); PIN(o0); PIN(o1); PIN(a1);
    v4u f0, f1;
#pragma nounroll
    for (int it = 0; it < SPIN; ++it) {
      CBAR();
      f0 = LD16(rws, o0, WS_FP + v * 64, SC1);
      f1 = LD16(rws, o1, WS_FP + v * 64 + 16 * 2560, SC1);
      if (none(umax(mx4(f0), mx4(f1)) == SENT)) break;
    }
    TS(10);
    const f32x4 z4 = {0.f, 0.f, 0.f, 0.f};
    const float mo = red(F4(f0) + (part < 11 ? F4(f1) : z4));
    const float res = x1 + rbf(dgb * rbf(mo));
    if (c < 4) ((__bf16*)out)[v * 16 + q4 * 4 + g] = (__bf16)res;
    if (v == 0 && lane == 0) ST8((v2u{elo, ehi}), rws, 0, WS_EPOCH, SC1);
    TS(11);
  }
  TS_FLUSH();
}

extern "C" void kernel_launch(void* const* d_in, const int* in_sizes, int n_in, void* d_out, int out_size, void* d_ws, size_t ws_size, hipStream_t stream) {
  fused_kernel<<<256, 512, 0, stream>>>((const u16*)d_in[1], (const u16*)d_in[2], (char*)d_ws, (const u16*)d_in[9], (const u16*)d_in[8], (const u16*)d_in[0],
      (const u16*)d_in[3], (const u16*)d_in[4], (const u16*)d_in[6], (const u16*)d_in[7], (u16*)d_out);
}
```

```cpp
#define KV_W1 3
#define KV_N1 4
#define KV_WIN 8
#include <hip/hip_runtime.h>
#include <hip/hip_bf16.h>
#include <stdint.h>

typedef unsigned short u16;
typedef unsigned v4u __attribute__((ext_vector_type(4)));
typedef unsigned v2u __attribute__((ext_vector_type(2)));
typedef float f32x4 __attribute__((ext_vector_type(4)));
typedef float f32x2 __attribute__((ext_vector_type(2)));
typedef __bf16 bf16x8 __attribute__((ext_vector_type(8)));
typedef __bf16 bf16x2 __attribute__((ext_vector_type(2)));
#define LAS __attribute__((address_space(3)))
#define DI __device__ __forceinline__
constexpr unsigned SENT = 0xFFFFFFFFu;
constexpr int SPIN = 200000;
constexpr int OOB = 0x7fffff00;
DI float bflo(unsigned u) { return __uint_as_float(u << 16); }
DI float bfhi(unsigned u) { return __uint_as_float(u & 0xffff0000u); }
DI unsigned cvtpk(float lo, float hi) { bf16x2 v = {(__bf16)lo, (__bf16)hi}; return __builtin_bit_cast(unsigned, v); }
DI float rbf(float f) { return (float)(__bf16)f; }
DI f32x4 mfma(v4u a, v4u b, f32x4 c) { return __builtin_amdgcn_mfma_f32_16x16x32_bf16(__builtin_bit_cast(bf16x8, a), __builtin_bit_cast(bf16x8, b), c, 0, 0, 0); }
DI unsigned umax(unsigned a, unsigned b) { return a > b ? a : b; }
DI unsigned nosentf(float f) { const unsigned u = __float_as_uint(f); return u < 0xFFFFFFFEu ? u : 0xFFFFFFFEu; }
DI unsigned mx4(v4u v) { return umax(umax(v.x, v.y), umax(v.z, v.w)); }
DI bool none(bool p) { return __builtin_amdgcn_ballot_w64(p) == 0ull; }
#define CBAR() asm volatile("" ::: "memory")
#define LGKM0() asm volatile("s_waitcnt lgkmcnt(0)" ::: "memory")
#define SB() __builtin_amdgcn_sched_barrier(0)
#define PIN(x) asm volatile("" : "+v"(x))
#define F4(v) __builtin_bit_cast(f32x4, v)
#define U2F(u) __uint_as_float(u)
#define F2U(f) __float_as_uint(f)
template <int CTRL> DI float dpp(float x) { return __builtin_bit_cast(float, __builtin_amdgcn_mov_dpp(__builtin_bit_cast(int, x), CTRL, 0xf, 0xf, true)); }
constexpr int XOR1 = 0xB1, XOR2 = 0x4E, XOR7 = 0x141, ROR8 = 0x128, ROR4 = 0x124, BCAST = 0x150;
DI float sw16(float a, float b) { auto s = __builtin_amdgcn_permlane16_swap(F2U(a), F2U(b), false, false); return U2F(s[0]) + U2F(s[1]); }
DI float sw32(float a, float b) { auto s = __builtin_amdgcn_permlane32_swap(F2U(a), F2U(b), false, false); return U2F(s[0]) + U2F(s[1]); }
DI float x16sum(float x) { return sw16(x, x); }
DI float x32sum(float x) { return sw32(x, x); }
DI float x16max(float x) { auto s = __builtin_amdgcn_permlane16_swap(F2U(x), F2U(x), false, false); return fmaxf(U2F(s[0]), U2F(s[1])); }
DI float x32max(float x) { auto s = __builtin_amdgcn_permlane32_swap(F2U(x), F2U(x), false, false); return fmaxf(U2F(s[0]), U2F(s[1])); }
DI float rsum(float x) { x += dpp<XOR1>(x); x += dpp<XOR2>(x); x += dpp<XOR7>(x); x += dpp<ROR8>(x); return x; }
DI float wsum(float x) { return x32sum(x16sum(rsum(x))); }
DI float wmax(float x) { x = fmaxf(x, dpp<XOR1>(x)); x = fmaxf(x, dpp<XOR2>(x)); x = fmaxf(x, dpp<XOR7>(x)); x = fmaxf(x, dpp<ROR8>(x)); return x32max(x16max(x)); }
typedef __amdgpu_buffer_rsrc_t rsrc_t;
DI rsrc_t mkrs(const void* p, int bytes) { return __builtin_amdgcn_make_buffer_rsrc((void*)p, 0, bytes, 0x00020000); }
#define LD16(r, vo, so, aux) __builtin_amdgcn_raw_buffer_load_b128((r), (vo), (so), (aux))
#define LD8(r, vo, so, aux) __builtin_amdgcn_raw_buffer_load_b64((r), (vo), (so), (aux))
#define LD4(r, vo, so, aux) __builtin_amdgcn_raw_buffer_load_b32((r), (vo), (so), (aux))
#define ST16(v, r, vo, so, aux) __builtin_amdgcn_raw_buffer_store_b128((v), (r), (vo), (so), (aux))
#define ST8(v, r, vo, so, aux) __builtin_amdgcn_raw_buffer_store_b64((v), (r), (vo), (so), (aux))
#define ST4(v, r, vo, so, aux) __builtin_amdgcn_raw_buffer_store_b32((v), (r), (vo), (so), (aux))
constexpr int NT = 2, SC1 = 16;
DI void lds_wait(volatile LAS unsigned* f, unsigned want) {
#pragma nounroll
  for (int i = 0; i < SPIN * 8; ++i) { if (__builtin_amdgcn_readfirstlane(*f) >= want) break; __builtin_amdgcn_s_sleep(1); }
  CBAR();
}
DI void lds_arrive(LAS unsigned* c, int lane) {
  LGKM0();
  if (lane == 0) __hip_atomic_fetch_add(c, 1u, __ATOMIC_RELAXED, __HIP_MEMORY_SCOPE_WORKGROUP);
  CBAR();
}
#define BARRIER() do { LGKM0(); __builtin_amdgcn_s_barrier(); CBAR(); } while (0)
#define VMW(n) asm volatile("s_waitcnt vmcnt(%0)" :: "n"(n) : "memory")
DI v4u selfrag(unsigned p, const v4u& mk) { return v4u{p & mk.x, p & mk.y, p & mk.z, p & mk.w}; }
DI v4u selmask(int c) { const int r7 = c & 7; const unsigned hm = (r7 & 1) ? 0xffff0000u : 0x0000ffffu;
  return v4u{(r7 >> 1) == 0 ? hm : 0u, (r7 >> 1) == 1 ? hm : 0u, (r7 >> 1) == 2 ? hm : 0u, (r7 >> 1) == 3 ? hm : 0u}; }
DI unsigned permlo(unsigned a, unsigned b) { return __builtin_amdgcn_perm(b, a, 0x05040100u); }
DI unsigned permhi(unsigned a, unsigned b) { return __builtin_amdgcn_perm(b, a, 0x07060302u); }
template <int N> DI unsigned badtags(const v4u (&u)[N], unsigned elo, unsigned ehi) {
  unsigned bad = 0;
#pragma unroll
  for (int k = 0; k < N; ++k) bad |= (u[k].y ^ elo) | (u[k].w ^ ehi);
  return bad;
}

constexpr int D = 640, HQ = 32, SK = 16384, DH = 128, WIN = 4096, DFF = 1728;
#define SV_Q1 2
#define STAMP 0
constexpr int WS_EPOCH = 0;
constexpr int WS_MP = 4096;
constexpr int WS_QF = WS_MP + 61440;
constexpr int WS_OP = WS_QF + 16384;
constexpr int WS_YP = WS_OP + 122880;
constexpr int WS_FP = WS_YP + 20480;
constexpr int WS_BYTES = WS_FP + 69120;
constexpr int WS_STAMP = 1 << 20;
constexpr int L_K = 0;
constexpr int L_P = 147456;
constexpr int L_Q = L_P + 6656;
constexpr int L_OM = L_Q + 256;
constexpr int L_F = L_OM + 4096;
constexpr int L_END = L_F + 256;
enum { F_KA = 0  , F_KB = 4  , F_FREE = 8  , F_QRDY = 12, F_CML = 13, F_M = 16  , F_L = 20   };
constexpr int S_W1 = 0;
constexpr int S_MB = 81920;
constexpr int S_S = S_MB + 12288;
constexpr int S_HT = S_S + 1280;
constexpr int S_Y = S_HT + 2560;
constexpr int S_Z = S_Y + 4096;
constexpr int S_X1 = S_Z + 2560;
constexpr int S_H = S_X1 + 3072;
constexpr int S_ST = S_H + 128;
static_assert(S_ST + 64 <= L_END, "");

#define TS(k) do {} while (0)
#define TS_FLUSH() do {} while (0)
#define DMA(m0_, vo_, srd_, so_) do { unsigned keep_; \
    asm volatile("s_mov_b32 %0, m0\n\ts_mov_b32 m0, %1\n\ts_nop 0\n\tbuffer_load_dwordx4 %2, %3, %4 offen nt lds\n\ts_mov_b32 m0, %0" \
                 : "=&s"(keep_) : "s"(m0_), "v"(vo_), "s"(srd_), "s"(so_) : "memory"); } while (0)

__global__ void __launch_bounds__(512, 2) fused_kernel(const u16* kc, const u16* vc, char* ws, const u16* wmod, const u16* cvec, const u16* x,
    const u16* wq, const u16* wo, const u16* w1, const u16* w2, u16* out) {
  __shared__ __attribute__((aligned(16))) char smem[L_END];
  LAS char* L = (LAS char*)smem;
  const int tid = threadIdx.x, lane = tid & 63, wave = __builtin_amdgcn_readfirstlane(tid >> 6);
  const int b = blockIdx.x, xg = b & 7, r = b >> 3;
  const int g = lane >> 4, c = lane & 15;
  TS(0);
  const unsigned lds0 = (unsigned)(uintptr_t)L;
  if (wave == 7) {
    unsigned long long pc; unsigned len;
    asm volatile("s_getpc_b64 %0\n.Lpf_here:\n\ts_mov_b32 %1, .Lpf_end-.Lpf_here" : "=s"(pc), "=s"(len));
    const rsrc_t rc_ = mkrs((const void*)pc, (int)len);
    unsigned t_ = LD4(rc_, lane * 128, (r & 3) * 8192, 0);
    asm volatile("" :: "v"(t_));
  }

  if (__builtin_expect(r < 24, 1)) {
    volatile LAS unsigned* FL = (volatile LAS unsigned*)(L + L_F);
    const int hd = xg * 4 + (r & 3), sp = r >> 2, j = wave & 3;
    const int nr = sp < 4 ? 43 : 42, r0 = sp * 43 - (sp == 5 ? 1 : 0);
    const size_t kvoff = ((size_t)hd * SK + (SK - WIN) + r0 * 16) * DH;
    if (__builtin_expect(wave < 4, 1)) {
      const unsigned long long ka = (unsigned long long)(kc + kvoff);
      const v4u srdK = {(unsigned)ka, (unsigned)(ka >> 32), (unsigned)nr * 4096u, 0x00020000u};
      int vo[4];
#pragma unroll
      for (int q = 0; q < 4; ++q) vo[q] = g * 256 + ((c ^ (4 * q + g)) * 16);
      const unsigned lbase = lds0 + L_K + j * 36864;
      asm volatile("s_nop 4" ::: "memory");
#pragma nounroll
      for (int i4 = 0; i4 < 11; ++i4) {
        if (i4 == 9) lds_wait(FL + F_FREE + j, 1);
        const unsigned mb = lbase + (i4 < 9 ? i4 : i4 - 9) * 4096, sb = i4 * 16384 + j * 1024;
#pragma unroll
        for (int q = 0; q < 4; ++q) { DMA(mb + q * 1024, vo[q], srdK, sb + q * 4096); if (i4 < KV_N1) VMW(KV_W1); else VMW(KV_WIN); }
        if (i4 == 1) __builtin_amdgcn_s_barrier();
        if (i4 == (16 + KV_WIN + 3) / 4 - 1) FL[F_KA + j] = 1u;
      }
      TS(1);
      const rsrc_t rws = mkrs(ws, WS_BYTES);
      const rsrc_t rv = mkrs(vc + kvoff, nr * 4096);
      const v2u ep = LD8(rws, 0, WS_EPOCH, SC1);
      v4u V[43];
#pragma unroll
      for (int i = 0; i < 43; ++i) {
        V[i] = LD16(rv, lane * 16, i * 4096 + j * 1024, NT);
        VMW(KV_WIN);
        if (i == KV_WIN - 1) { FL[F_KB + j] = 1u; TS(2); }
        SB();
      }
      TS(3);
      lds_wait(FL + F_CML, 4);
      TS(4);
      const unsigned long long e1 = (((unsigned long long)ep.y << 32) | ep.x) + 1ull;
      unsigned elo = (unsigned)e1, ehi = (unsigned)(e1 >> 32);
      const v4u mk = selmask(c);
      const float m0_ = U2F(FL[F_M]), m1_ = U2F(FL[F_M + 1]), m2_ = U2F(FL[F_M + 2]), m3_ = U2F(FL[F_M + 3]);
      float mcu = fmaxf(fmaxf(m0_, m1_), fmaxf(m2_, m3_));
      float lcu = U2F(FL[F_L]) * __expf(m0_ - mcu) + U2F(FL[F_L + 1]) * __expf(m1_ - mcu) + U2F(FL[F_L + 2]) * __expf(m2_ - mcu) + U2F(FL[F_L + 3]) * __expf(m3_ - mcu);
      float fj = __expf(U2F(FL[F_M + j]) - mcu) / lcu;
      int sto = lane < 32 ? c * 32 + g * 16 : (lane == 32 ? 512 : OOB);
      PIN(fj); PIN(mcu); PIN(lcu); PIN(sto); PIN(elo); PIN(ehi);
      const LAS v4u* pt = (const LAS v4u*)(L + L_P + (((j * 2 + (c >> 3)) * 4 + g) * 52) * 4);
      v4u p4[11];
#pragma unroll
      for (int i4 = 0; i4 < 11; ++i4) p4[i4] = pt[i4];
      SB();
      f32x4 acc = {0.f, 0.f, 0.f, 0.f};
#pragma unroll
      for (int i = 0; i < 43; ++i) acc = mfma(selfrag(p4[i >> 2][i & 3], mk), V[i], acc);
      ((LAS f32x4*)(L + L_OM))[j * 64 + lane] = acc * fj;
      TS(5);
      BARRIER();
      if (j == 0) {
        const LAS f32x4* om = (const LAS f32x4*)(L + L_OM) + (lane & 31);
        const f32x4 o = ((om[0] + om[32]) + (om[64] + om[96])) + ((om[128] + om[160]) + (om[192] + om[224]));
        const bool ml = lane >= 32;
        const v4u st = {ml ? F2U(mcu) : cvtpk(o.x, o.y), elo, ml ? F2U(lcu) : cvtpk(o.z, o.w), ehi};
        ST16(st, rws, sto, WS_OP + (hd * 6 + sp) * 640, SC1);
        TS(6);
      }
      TS_FLUSH();
      return;
    }
    FL[lane] = 0u;
    BARRIER();
    const LAS char* kb = L + L_K + j * 36864 + c * 256;
    v4u kf[11][4];
    lds_wait(FL + F_KA + j, 1);
#pragma unroll
    for (int t = 0; t < 4; ++t)
#pragma unroll
      for (int ks = 0; ks < 4; ++ks) kf[t][ks] = *(const LAS v4u*)(kb + t * 4096 + (((4 * ks + g) ^ c) * 16));
    SB();
    LGKM0();
    FL[F_FREE + j] = 1u;
    TS(1);
    if (j == 0) {
      const rsrc_t rws = mkrs(ws, WS_BYTES);
      const v2u ep = LD8(rws, 0, WS_EPOCH, SC1);
      const unsigned long long e1 = (((unsigned long long)ep.y << 32) | ep.x) + 1ull;
      const unsigned elo = (unsigned)e1, ehi = (unsigned)(e1 >> 32);
      v4u qu[1];
#pragma nounroll
      for (int it = 0; it < SPIN; ++it) {
        CBAR();
        qu[0] = LD16(rws, (lane & 31) * 16, WS_QF + hd * 512, SC1);
        if (none(badtags<1>(qu, elo, ehi) != 0u)) break;
      }
      ((LAS v2u*)(L + L_Q))[lane & 31] = v2u{qu[0].x, qu[0].z};
      LGKM0();
      FL[F_QRDY] = 1u;
    }
    TS(2);
    lds_wait(FL + F_KB + j, 1);
#pragma unroll
    for (int t = 4; t < 11; ++t)
#pragma unroll
      for (int ks = 0; ks < 4; ++ks) kf[t][ks] = *(const LAS v4u*)(kb + (t < 9 ? t : t - 9) * 4096 + (((4 * ks + g) ^ c) * 16));
    SB();
    lds_wait(FL + F_QRDY, 1);
    TS(3);
    v4u qa[4];
#pragma unroll
    for (int ks = 0; ks < 4; ++ks) qa[ks] = *(const LAS v4u*)(L + L_Q + (4 * ks + g) * 16);
    float sc[11];
#pragma unroll
    for (int t = 0; t < 11; ++t) {
      f32x4 d = {0.f, 0.f, 0.f, 0.f};
#pragma unroll
      for (int ks = 0; ks < 4; ++ks) d = mfma(qa[ks], kf[t][ks], d);
      sc[t] = d.x;
    }
#pragma unroll
    for (int t = 0; t < 11; ++t) PIN(sc[t]);
    const bool g1 = g & 1, g2 = g & 2;
    float s0 = g2 ? (g1 ? sc[3] : sc[2]) : (g1 ? sc[1] : sc[0]);
    float s1 = g2 ? (g1 ? sc[7] : sc[6]) : (g1 ? sc[5] : sc[4]);
    float s2 = g2 ? sc[10] : (g1 ? sc[9] : sc[8]);
    s0 = rbf(rbf(s0) * 0.08838834764831845f); s1 = rbf(rbf(s1) * 0.08838834764831845f); s2 = rbf(rbf(s2) * 0.08838834764831845f);
    const bool v2 = 128 + lane < 4 * nr;
    if (!v2) s2 = -INFINITY;
    const float m = wmax(fmaxf(fmaxf(s0, s1), s2));
    const float p0 = __expf(s0 - m), p1 = __expf(s1 - m), p2 = v2 ? __expf(s2 - m) : 0.f;
    const float l = wsum((p0 + p1) + p2);
    const unsigned h0 = cvtpk(p0, p0), h1 = cvtpk(p1, p1), h2 = cvtpk(p2, p2);
    const float q0 = p0 - bflo(h0), q1 = p1 - bflo(h1), q2 = p2 - bflo(h2);
    LAS unsigned* pw = (LAS unsigned*)(L + L_P) + (j * 8 + (lane & 3)) * 52 + (lane >> 2);
    pw[0] = h0; pw[16] = h1; pw[32] = h2; pw[208] = cvtpk(q0, q0); pw[224] = cvtpk(q1, q1); pw[240] = cvtpk(q2, q2);
    if (lane == 0) { FL[F_M + j] = F2U(m); FL[F_L + j] = F2U(l); }
    lds_arrive((LAS unsigned*)(L + L_F) + F_CML, lane);
    TS(4);
    TS_FLUSH();
    return;
  }

  const int w = wave, k = r - 24, sid = xg * 8 + k;
  const bool role_b = k < 5, role_c = k >= 5 || (k == 0 && xg < 3), role_m = !(k == 0 && xg < 4);
  const int uc = k >= 5 ? xg * 3 + k - 5 : 24 + xg;
  const int mi = xg < 4 ? xg * 7 + k - 1 : 28 + (xg - 4) * 8 + k;
  const int chi = c & 7, rho = c >> 3, kh = mi & 1;
  const rsrc_t rws = mkrs(ws, WS_BYTES);
  const rsrc_t rm = mkrs(wmod, role_m ? D * 6 * D * 2 : 0);
  const rsrc_t rx = mkrs(x, D * 2);
  const rsrc_t rq = mkrs(wq, D * HQ * DH * 2);
  const int mvo = (5 * (2 * g + rho)) * 7680 + chi * 16, mso = (320 * kh + 40 * w) * 7680 + (mi >> 1) * 128;
  const int qvo = (10 * (2 * g + rho)) * 8192 + chi * 16, qso = (80 * w) * 8192 + sid * 128;
  v4u WM[5], WQ[10];
  const v2u cv = LD8(mkrs(cvec, D * 2), (lane < 10 ? lane : 0) * 8, kh * 640 + w * 80, 0);
  v2u ep = LD8(rws, 0, WS_EPOCH, SC1);
#pragma unroll
  for (int i = 0; i < 5; ++i) WM[i] = LD16(rm, mvo, mso + i * 7680, NT);
  SB(); CBAR();
  __builtin_amdgcn_s_barrier();
  CBAR();
#pragma unroll
  for (int i = 0; i < SV_Q1; ++i) WQ[i] = LD16(rq, qvo, qso + i * 8192, NT);
  SB(); CBAR();
  const v4u mk = selmask(c);
  unsigned sv5[5];
  {
    float cf[4] = {bflo(cv.x), bfhi(cv.x), bflo(cv.y), bfhi(cv.y)};
    unsigned sd[4];
#pragma unroll
    for (int i = 0; i < 4; ++i) { const float sv = cf[i] / (1.f + __expf(-cf[i])); sd[i] = cvtpk(sv, sv); }
    if (lane < 10) ((LAS v4u*)(L + S_S))[w * 10 + lane] = v4u{sd[0], sd[1], sd[2], sd[3]};
    LGKM0();
#pragma unroll
    for (int i = 0; i < 5; ++i) sv5[i] = ((const LAS unsigned*)(L + S_S))[w * 40 + 5 * (2 * g + rho) + i];
  }
  PIN(ep);
  const unsigned long long e1 = (((unsigned long long)ep.y << 32) | ep.x) + 1ull;
  const unsigned elo = (unsigned)e1, ehi = (unsigned)(e1 >> 32);
  const int mbw = ((w * 2 + rho) * 16 + chi * 2 + (g & 1)) * 16;
  auto merge16 = [&](int base) {
    const LAS f32x2* mb = (const LAS f32x2*)(L + base) + (lane & 31);
    f32x2 t[16];
#pragma unroll
    for (int i = 0; i < 16; ++i) t[i] = mb[32 * i];
    return (((t[0] + t[1]) + (t[2] + t[3])) + ((t[4] + t[5]) + (t[6] + t[7]))) + (((t[8] + t[9]) + (t[10] + t[11])) + ((t[12] + t[13]) + (t[14] + t[15])));
  };
  auto mod_unit = [&](int base, int um) {
    f32x4 acc = {0.f, 0.f, 0.f, 0.f};
#pragma unroll
    for (int i = 0; i < 5; ++i) acc = mfma(selfrag(sv5[i], mk), WM[i], acc);
    if ((g >> 1) == rho) *(LAS f32x4*)(L + base + mbw) = acc;
    BARRIER();
    if (w == 0) { const f32x2 mv = merge16(base); ST16((v4u{F2U(mv.x), elo, F2U(mv.y), ehi}), rws, (lane < 32 && role_m) ? lane * 16 : OOB, WS_MP + kh * 30720 + (um >> 1) * 512, SC1); }
  };
  mod_unit(S_MB, mi);
  TS(1);
  unsigned xr[5];
#pragma unroll
  for (int i = 0; i < 5; ++i) xr[i] = LD4(rx, lane * 4, i * 256, 0);
  const unsigned xme = LD4(rx, (lane < 40 ? lane : 0) * 4, w * 160, 0);
  ST16((v4u{SENT, SENT, SENT, SENT}), rws, w == 1 ? ((role_b && lane < 32) ? WS_YP + xg * 2560 + k * 512 + lane * 16 : OOB)
                                          : ((role_c && w >= 2 && (w - 2) * 64 + lane < 160) ? WS_FP + uc * 2560 + (w - 2) * 1024 + lane * 16 : OOB), 0, SC1);
#pragma unroll
  for (int i = SV_Q1; i < 10; ++i) WQ[i] = LD16(rq, qvo, qso + i * 8192, NT);
  SB(); CBAR();
  {
    float s = 0.f;
#pragma unroll
    for (int i = 0; i < 5; ++i) s += bflo(xr[i]) + bfhi(xr[i]);
    float mean = wsum(s) * (1.f / D);
    float v = 0.f;
#pragma unroll
    for (int i = 0; i < 5; ++i) { const float d0 = bflo(xr[i]) - mean, d1 = bfhi(xr[i]) - mean; v += d0 * d0 + d1 * d1; }
    v = wsum(v) * (1.f / D);
    const float den = rbf(sqrtf(rbf(rbf(v) + 1e-5f)));
    mean = rbf(mean);
    float n0 = rbf(rbf(bflo(xme) - mean) / den), n1 = rbf(rbf(bfhi(xme) - mean) / den);
    PIN(n0); PIN(n1);
    const int vo_ = (lane < 40 ? lane : 0) * 16 + w * 640;
    v4u u[4];
    u[0] = LD16(rws, vo_, WS_MP, SC1); u[1] = LD16(rws, vo_, WS_MP + 30720, SC1); u[2] = LD16(rws, vo_, WS_MP + 5120, SC1); u[3] = LD16(rws, vo_, WS_MP + 35840, SC1);
#pragma unroll
    for (int i = 0; i < 5; ++i) WM[i] = LD16(rm, mvo, mso + 30 * 128 + i * 7680, NT);
    SB(); CBAR();
    if (!none(badtags<4>(u, elo, ehi) != 0u)) {
#pragma nounroll
      for (int it = 0; it < SPIN; ++it) {
        CBAR();
        u[0] = LD16(rws, vo_, WS_MP, SC1); u[1] = LD16(rws, vo_, WS_MP + 30720, SC1); u[2] = LD16(rws, vo_, WS_MP + 5120, SC1); u[3] = LD16(rws, vo_, WS_MP + 35840, SC1);
        if (none(badtags<4>(u, elo, ehi) != 0u)) break;
      }
    }
    TS(2);
    const float sa0 = rbf(U2F(u[0].x) + U2F(u[1].x)), sa1 = rbf(U2F(u[0].z) + U2F(u[1].z)), ca0 = rbf(U2F(u[2].x) + U2F(u[3].x)), ca1 = rbf(U2F(u[2].z) + U2F(u[3].z));
    const float h0 = rbf(n0 * rbf(1.f + ca0)) + sa0, h1 = rbf(n1 * rbf(1.f + ca1)) + sa1;
    if (lane < 40) ((LAS v2u*)(L + S_HT))[w * 40 + lane] = v2u{cvtpk(h0, h0), cvtpk(h1, h1)};
    LGKM0();
    const LAS v2u* ht = (const LAS v2u*)(L + S_HT) + w * 40 + 5 * (2 * g + rho);
    f32x4 acc = {0.f, 0.f, 0.f, 0.f};
#pragma unroll
    for (int i = 0; i < 5; ++i) { const v2u h2 = ht[i]; acc = mfma(selfrag(h2.x, mk), WQ[2 * i], acc); acc = mfma(selfrag(h2.y, mk), WQ[2 * i + 1], acc); }
    if ((g >> 1) == rho) *(LAS f32x4*)(L + S_MB + 4096 + mbw) = acc;
    BARRIER();
    if (w == 0) {
      const f32x2 qv = merge16(S_MB + 4096);
      const unsigned qp = cvtpk(qv.x, qv.y), qn = F2U(dpp<XOR1>(U2F(qp)));
      ST16((v4u{qp, elo, qn, ehi}), rws, (lane < 32 && !(lane & 1)) ? lane * 8 : OOB, WS_QF + sid * 256, SC1);
    }
  }
  TS(3);
  const int hb = xg * 4 + (w >> 1), kb = w & 1;
  v4u BF[2][8];
  v4u B2[2][2][4];
  v2u W2r[2][16];
  const rsrc_t r2 = mkrs(w2, DFF * D * 2);
  auto issue_w2 = [&]() {
#pragma unroll
    for (int i = 0; i < 16; ++i) W2r[0][i] = LD8(r2, (8 * g) * 1280 + c * 8, (64 * uc + (i >> 3) * 32 + (i & 7)) * 1280 + w * 128, NT);
    if (w < 2) {
#pragma unroll
      for (int i = 0; i < 16; ++i) W2r[1][i] = LD8(r2, (8 * g) * 1280 + c * 8, (64 * uc + (i >> 3) * 32 + (i & 7)) * 1280 + (8 + w) * 128, NT);
    }
    SB(); CBAR();
  };
  auto issue_w1 = [&](int i0, int i1) {
    const unsigned long long wa = (unsigned long long)w1;
    const v4u srd1 = {(unsigned)wa, (unsigned)(wa >> 32), (unsigned)(D * DFF * 2), 0x00020000u};
    const int f0 = (g & 1) | ((w >> 2) << 2);
    const int voa = (lane >> 3) * 3456 + (((lane & 7) ^ f0) * 16), vob = (lane >> 3) * 3456 + (((lane & 7) ^ f0 ^ 2) * 16);
    asm volatile("s_nop 4" ::: "memory");
#pragma nounroll
    for (int i = i0; i < i1; i += 2) {
      const unsigned m0v = lds0 + S_W1 + (w * 10 + i) * 1024, so = (w * 80 + i * 8) * 3456 + uc * 128;
      DMA(m0v, voa, srd1, so); DMA(m0v + 1024, vob, srd1, so + 8 * 3456);
    }
  };
#pragma unroll
  for (int i = 0; i < 5; ++i) PIN(WM[i]);
  if (role_b) {
    v4u WB[16];
    const rsrc_t ro = mkrs(wo, HQ * DH * D * 2);
#define LDWB(i) WB[i] = LD16(ro, (4 * g) * 1280 + c * 16, (hb * 128 + 64 * kb + 32 * (((i) >> 2) & 1) + 16 * ((i) >> 3) + ((i) & 3)) * 1280 + k * 256, NT)
#pragma unroll
    for (int i = 0; i < 3; ++i) LDWB(i);
    SB(); CBAR();
    mod_unit(S_MB + 8192, 60 + mi);
    TS(4);
#pragma unroll
    for (int i = 3; i < 16; ++i) LDWB(i);
    SB(); CBAR();
    if (role_c) issue_w1(0, 10);
#pragma unroll
    for (int hf = 0; hf < 2; ++hf) {
#pragma unroll
      for (int m = 0; m < 4; ++m)
#pragma unroll
        for (int d = 0; d < 4; ++d) { BF[hf][2 * d][m] = permlo(WB[hf * 8 + 2 * m][d], WB[hf * 8 + 2 * m + 1][d]); BF[hf][2 * d + 1][m] = permhi(WB[hf * 8 + 2 * m][d], WB[hf * 8 + 2 * m + 1][d]); }
#pragma unroll
      for (int t = 0; t < 8; ++t) PIN(BF[hf][t]);
    }
  } else {
    issue_w1(0, 4);
    mod_unit(S_MB + 8192, 60 + mi);
    TS(4);
    issue_w1(4, 10);
  }
  v4u B1[10];
  f32x2 zg = {0.f, 0.f}, zx = {0.f, 0.f}, ga2 = {0.f, 0.f}, x2 = {0.f, 0.f};
  float Cc = 0.f, Ss = 0.f;
  const int eo = (lane < 40 ? lane : 0) * 8 + w * 320;
  const LAS char* zr = L + S_Z + (c & 1) * 640 + g * 16;
  if (role_c) {
    v4u mm[7];
#pragma nounroll
    for (int it = 0; it < SPIN; ++it) {
      CBAR();
#pragma unroll
      for (int i = 0; i < 6; ++i) mm[i] = LD16(rws, eo * 2, WS_MP + (i & 1) * 30720 + (2 + (i >> 1)) * 5120, SC1);
      mm[6] = LD16(rws, (lane & 7) * 256, WS_QF + w * 2048, SC1);
      if (none(badtags<7>(mm, elo, ehi) != 0u)) break;
      __builtin_amdgcn_s_sleep(8);
    }
    ga2 = f32x2{rbf(U2F(mm[0].x) + U2F(mm[1].x)), rbf(U2F(mm[0].z) + U2F(mm[1].z))};
    const f32x2 c1 = {rbf(1.f + rbf(U2F(mm[4].x) + U2F(mm[5].x))), rbf(1.f + rbf(U2F(mm[4].z) + U2F(mm[5].z)))};
    x2 = f32x2{bflo(xme), bfhi(xme)};
    zg = ga2 * c1; zx = x2 * c1;
    if (lane < 40) { *(LAS unsigned*)(L + S_Z + (eo >> 1)) = cvtpk(c1.x, c1.y); *(LAS unsigned*)(L + S_Z + 1280 + (eo >> 1)) = cvtpk(U2F(mm[2].x) + U2F(mm[3].x), U2F(mm[2].z) + U2F(mm[3].z)); }
    if (w == 0) *(LAS f32x2*)(L + S_X1 + 2560 + lane * 8) = f32x2{0.f, 0.f};
    issue_w2();
    BARRIER();
    {
      typedef short v4s __attribute__((ext_vector_type(4)));
      const int q_ = c >> 2, p_ = c & 3, f_ = (q_ >> 1) | ((g & 1) << 1) | ((p_ >> 1) << 2);
      LAS char* wi = L + S_W1 + (320 * (p_ >> 1) + 8 * g + q_) * 128 + ((w ^ f_) * 16) + (p_ & 1) * 8;
#pragma unroll
      for (int ks = 0; ks < 10; ++ks) {
        const v2u lo = __builtin_bit_cast(v2u, __builtin_amdgcn_ds_read_tr16_b64_v4i16((LAS v4s*)(wi + ks * 4096)));
        const v2u hi = __builtin_bit_cast(v2u, __builtin_amdgcn_ds_read_tr16_b64_v4i16((LAS v4s*)(wi + ks * 4096 + 512)));
        B1[ks] = v4u{lo.x, lo.y, hi.x, hi.y};
      }
#pragma unroll
      for (int ks = 0; ks < 10; ++ks) PIN(B1[ks]);
    }
    const LAS char* zr2 = zr + ((c >> 1) & 1) * 1280;
    f32x4 d0 = {0.f, 0.f, 0.f, 0.f};
#pragma unroll
    for (int ks = 0; ks < 10; ++ks) d0 = mfma(*(const LAS v4u*)(zr2 + ks * 64), B1[ks], d0);
    Cc = d0.x + dpp<ROR8>(d0.y); Ss = d0.z + dpp<ROR8>(d0.w);
#pragma unroll
    for (int tk = 0; tk < 2; ++tk)
#pragma unroll
      for (int ks = 0; ks < 2; ++ks) {
#pragma unroll
        for (int m = 0; m < 4; ++m) {
          const v2u e = W2r[tk][ks * 8 + 2 * m], o = W2r[tk][ks * 8 + 2 * m + 1];
          B2[tk][ks][0][m] = permlo(e.x, o.x); B2[tk][ks][1][m] = permhi(e.x, o.x); B2[tk][ks][2][m] = permlo(e.y, o.y); B2[tk][ks][3][m] = permhi(e.y, o.y);
        }
#pragma unroll
        for (int t = 0; t < 4; ++t) PIN(B2[tk][ks][t]);
      }
    PIN(Cc); PIN(Ss); PIN(zg); PIN(zx); PIN(ga2); PIN(x2);
    BARRIER();
  }
  const bool role_d = role_b && ((w == 1 && k > 0) || (w == 2 && k == 1));
  const int v = xg * 5 + (w == 1 ? k : 0);
  const int q4 = lane & 3, part = lane >> 2;
  float dx = 0.f, dga = 0.f, dgb = 0.f;
  if (role_d) {
    const unsigned xv = LD4(rx, q4 * 8 + (g >> 1) * 4, v * 32, 0);
    v4u mm[5];
#pragma nounroll
    for (int it = 0; it < SPIN; ++it) {
      CBAR();
#pragma unroll
      for (int i = 0; i < 4; ++i) mm[i] = LD16(rws, q4 * 32 + (g >> 1) * 16, WS_MP + (i & 1) * 30720 + (2 + 3 * (i >> 1)) * 5120 + v * 128, SC1);
      mm[4] = LD16(rws, lane * 256, WS_QF, SC1);
      if (none(badtags<5>(mm, elo, ehi) != 0u)) break;
      __builtin_amdgcn_s_sleep(8);
    }
    const bool odd = g & 1;
    dx = odd ? bfhi(xv) : bflo(xv);
    dga = rbf(odd ? U2F(mm[0].z) + U2F(mm[1].z) : U2F(mm[0].x) + U2F(mm[1].x));
    dgb = rbf(odd ? U2F(mm[2].z) + U2F(mm[3].z) : U2F(mm[2].x) + U2F(mm[3].x));
    PIN(dx); PIN(dga); PIN(dgb);
  }
  TS(5);
  if (role_b) {
    const int s1 = (c & 7) < 6 ? (c & 7) : 0, s2 = (c + 4 * g) & 7;
    int vo1 = s1 * 640 + kb * 256 + (c >> 3) * 64 + g * 16, vo2 = (s2 < 6 ? s2 : 0) * 640 + 512;
    unsigned m_lo = c < 8 ? ~0u : 0u, m_hi = ~m_lo;
    float live = s2 < 6 ? 1.f : 0.f;
    int lw = S_Y + (w * 128 + c * 8 + g) * 4;
    PIN(vo1); PIN(vo2); PIN(lw); PIN(m_lo); PIN(m_hi); PIN(live);
    v4u oa[3];
#pragma nounroll
    for (int it = 0; it < SPIN; ++it) {
      CBAR();
      oa[2] = LD16(rws, vo2, WS_OP + hb * 3840, SC1);
      oa[0] = LD16(rws, vo1, WS_OP + hb * 3840, SC1);
      oa[1] = LD16(rws, vo1, WS_OP + hb * 3840 + 128, SC1);
      if (none(badtags<3>(oa, elo, ehi) != 0u)) break;
    }
    TS(6);
    const v4u a0 = {oa[0].x & m_lo, oa[0].z & m_lo, oa[1].x & m_lo, oa[1].z & m_lo}, a1 = {oa[0].x & m_hi, oa[0].z & m_hi, oa[1].x & m_hi, oa[1].z & m_hi};
    f32x4 d[8];
#pragma unroll
    for (int t = 0; t < 8; ++t) d[t] = mfma(a0, BF[0][t], f32x4{0.f, 0.f, 0.f, 0.f});
#pragma unroll
    for (int t = 0; t < 8; ++t) d[t] = mfma(a1, BF[1][t], d[t]);
    const float ms = U2F(oa[2].x);
    float M = fmaxf(ms, dpp<XOR1>(ms)); M = fmaxf(M, dpp<XOR2>(M)); M = fmaxf(M, dpp<XOR7>(M));
    const float e = U2F(oa[2].z) * __expf(ms - M) * live;
    float Ls = e + dpp<XOR1>(e); Ls += dpp<XOR2>(Ls); Ls += dpp<XOR7>(Ls);
    const float wt = e * __builtin_amdgcn_rcpf(Ls);
    const float w0 = dpp<BCAST + 0>(wt), w1_ = dpp<BCAST + 1>(wt), w2_ = dpp<BCAST + 2>(wt), w3 = dpp<BCAST + 3>(wt);
    float y[8];
#pragma unroll
    for (int t = 0; t < 8; ++t) y[t] = (d[t].x * w0 + d[t].y * w1_) + (d[t].z * w2_ + d[t].w * w3);
    const float ya = sw32(sw16(y[0], y[1]), sw16(y[2], y[3])), yb = sw32(sw16(y[4], y[5]), sw16(y[6], y[7]));
    *(LAS float*)(L + lw) = ya; *(LAS float*)(L + lw + 16) = yb;
    BARRIER();
    if (w == 0) {
      const LAS f32x2* tp = (const LAS f32x2*)(L + S_Y) + lane;
      const f32x2 yy = ((tp[0] + tp[64]) + (tp[128] + tp[192])) + ((tp[256] + tp[320]) + (tp[384] + tp[448]));
      ST8((v2u{nosentf(yy.x), nosentf(yy.y)}), rws, lane * 8, WS_YP + xg * 2560 + k * 512, SC1);
    }
    TS(7);
  }
  if (role_c) {
    int zw = lane < 40 ? S_Z + (eo >> 1) : S_ST, xw = lane < 40 ? S_X1 + eo : S_ST + 8;
    int hw = lane < 8 ? S_H + (w * 8 + lane) * 2 : S_ST + 16;
    int sto = lane < 16 ? lane * 16 : OOB;
    PIN(zw); PIN(xw); PIN(hw); PIN(sto);
    f32x2 at;
#pragma nounroll
    for (int it = 0; it < SPIN; ++it) {
      CBAR();
      v2u yp[8];
#pragma unroll
      for (int i = 0; i < 8; ++i) yp[i] = LD8(rws, eo, WS_YP + i * 2560, SC1);
#define F2(v_) __builtin_bit_cast(f32x2, v_)
      at = ((F2(yp[0]) + F2(yp[1])) + (F2(yp[2]) + F2(yp[3]))) + ((F2(yp[4]) + F2(yp[5])) + (F2(yp[6]) + F2(yp[7])));
      if (__builtin_expect(none(__builtin_isunordered(at.x, at.y)), 1)) break;
      unsigned mx = 0;
#pragma unroll
      for (int i = 0; i < 8; ++i) mx = umax(mx, umax(yp[i].x, yp[i].y));
      if (none(mx == SENT)) break;
    }
    TS(8);
    const f32x2 z = zx + zg * at, x1 = x2 + ga2 * at;
    *(LAS unsigned*)(L + zw) = cvtpk(z.x, z.y);
    *(LAS f32x2*)(L + xw) = x1;
    BARRIER();
    v4u af[10];
#pragma unroll
    for (int ks = 0; ks < 10; ++ks) af[ks] = *(const LAS v4u*)(zr + ks * 64);
    const LAS f32x4* xp = (const LAS f32x4*)(L + S_X1) + lane;
    const f32x4 xa = xp[0], xb = xp[64], xc = xp[128];
    SB();
    f32x4 d0 = {0.f, 0.f, 0.f, 0.f}, d1 = {0.f, 0.f, 0.f, 0.f};
#pragma unroll
    for (int ks = 0; ks < 5; ++ks) { d0 = mfma(af[2 * ks], B1[2 * ks], d0); d1 = mfma(af[2 * ks + 1], B1[2 * ks + 1], d1); }
    const f32x4 t1 = (xa + xb) + xc, t2 = (xa * xa + xb * xb) + xc * xc;
    float s12 = sw16((t1.x + t1.y) + (t1.z + t1.w), (t2.x + t2.y) + (t2.z + t2.w));
    s12 = x32sum(rsum(s12));
    const float mean = U2F(__builtin_amdgcn_readlane(F2U(s12), 0)) * (1.f / D);
    const float var = fmaxf(U2F(__builtin_amdgcn_readlane(F2U(s12), 16)) * (1.f / D) - mean * mean, 0.f);
    const float rden = __builtin_amdgcn_rcpf(rbf(__builtin_amdgcn_sqrtf(rbf(rbf(var) + 1e-5f))));
    const float tt = Ss - Cc * (rbf(mean) * rden);
    const float uu = (d0.x + d1.x) + dpp<ROR8>(d0.y + d1.y);
    *(LAS __bf16*)(L + hw) = (__bf16)fmaxf(uu * rden + tt, 0.f);
    BARRIER();
    const v4u ha0 = *(const LAS v4u*)(L + S_H + g * 16), ha1 = *(const LAS v4u*)(L + S_H + 64 + g * 16);
    {
      f32x4 o[4];
#pragma unroll
      for (int t = 0; t < 4; ++t) o[t] = mfma(ha0, B2[0][0][t], f32x4{0.f, 0.f, 0.f, 0.f});
#pragma unroll
      for (int t = 0; t < 4; ++t) o[t] = mfma(ha1, B2[0][1][t], o[t]);
      ST16((v4u{nosentf(o[0].x), nosentf(o[1].x), nosentf(o[2].x), nosentf(o[3].x)}), rws, sto, WS_FP + uc * 2560 + w * 256, SC1);
    }
    if (w < 2) {
      f32x4 o[4];
#pragma unroll
      for (int t = 0; t < 4; ++t) o[t] = mfma(ha0, B2[1][0][t], f32x4{0.f, 0.f, 0.f, 0.f});
#pragma unroll
      for (int t = 0; t < 4; ++t) o[t] = mfma(ha1, B2[1][1][t], o[t]);
      ST16((v4u{nosentf(o[0].x), nosentf(o[1].x), nosentf(o[2].x), nosentf(o[3].x)}), rws, sto, WS_FP + uc * 2560 + (8 + w) * 256, SC1);
    }
    TS(9);
  }
  if (role_d) {
    v4u yq;
#pragma nounroll
    for (int it = 0; it < SPIN; ++it) {
      CBAR();
      yq = LD16(rws, q4 * 16 + (part & 7) * 2560, WS_YP + v * 64, SC1);
      if (none(mx4(yq) == SENT)) break;
    }
    auto red = [&](f32x4 t) { float s = sw32(sw16(t.x, t.y), sw16(t.z, t.w)); s += dpp<ROR4>(s); s += dpp<ROR8>(s); return s; };
    float x1 = rbf(dx + rbf(dga * rbf(0.5f * red(F4(yq)))));
    int o0 = q4 * 16 + part * 2560, o1 = q4 * 16 + (part < 11 ? part : 0) * 2560;
    float a1 = part < 11 ? 1.f : 0.f;
    PIN(x1); PIN(o0); PIN(o1); PIN(a1);
    v4u f0, f1;
#pragma nounroll
    for (int it = 0; it < SPIN; ++it) {
      CBAR();
      f0 = LD16(rws, o0, WS_FP + v * 64, SC1);
      f1 = LD16(rws, o1, WS_FP + v * 64 + 16 * 2560, SC1);
      if (none(umax(mx4(f0), mx4(f1)) == SENT)) break;
    }
    TS(10);
    const f32x4 z4 = {0.f, 0.f, 0.f, 0.f};
    const float mo = red(F4(f0) + (part < 11 ? F4(f1) : z4));
    const float res = x1 + rbf(dgb * rbf(mo));
    if (c < 4) ((__bf16*)out)[v * 16 + q4 * 4 + g] = (__bf16)res;
    if (v == 0 && lane == 0) ST8((v2u{elo, ehi}), rws, 0, WS_EPOCH, SC1);
    TS(11);
    asm volatile(".Lpf_end:");
  }
  TS_FLUSH();
}

extern "C" void kernel_launch(void* const* d_in, const int* in_sizes, int n_in, void* d_out, int out_size, void* d_ws, size_t ws_size, hipStream_t stream) {
  fused_kernel<<<256, 512, 0, stream>>>((const u16*)d_in[1], (const u16*)d_in[2], (char*)d_ws, (const u16*)d_in[9], (const u16*)d_in[8], (const u16*)d_in[0],
      (const u16*)d_in[3], (const u16*)d_in[4], (const u16*)d_in[6], (const u16*)d_in[7], (u16*)d_out);
}
```

```cpp
#define KV_W1 4
#define KV_N1 4
#define SV_W 6
#define T_POLLQ 600
#define T_POLL1 370
#define CODEPF 2
#include <hip/hip_runtime.h>
#include <hip/hip_bf16.h>
#include <stdint.h>

typedef unsigned short u16;
typedef unsigned v4u __attribute__((ext_vector_type(4)));
typedef unsigned v2u __attribute__((ext_vector_type(2)));
typedef float f32x4 __attribute__((ext_vector_type(4)));
typedef float f32x2 __attribute__((ext_vector_type(2)));
typedef __bf16 bf16x8 __attribute__((ext_vector_type(8)));
typedef __bf16 bf16x2 __attribute__((ext_vector_type(2)));
#define LAS __attribute__((address_space(3)))
#define DI __device__ __forceinline__
constexpr unsigned SENT = 0xFFFFFFFFu;
constexpr int SPIN = 200000;
constexpr int OOB = 0x7fffff00;
DI float bflo(unsigned u) { return __uint_as_float(u << 16); }
DI float bfhi(unsigned u) { return __uint_as_float(u & 0xffff0000u); }
DI unsigned cvtpk(float lo, float hi) { bf16x2 v = {(__bf16)lo, (__bf16)hi}; return __builtin_bit_cast(unsigned, v); }
DI float rbf(float f) { return (float)(__bf16)f; }
DI f32x4 mfma(v4u a, v4u b, f32x4 c) { return __builtin_amdgcn_mfma_f32_16x16x32_bf16(__builtin_bit_cast(bf16x8, a), __builtin_bit_cast(bf16x8, b), c, 0, 0, 0); }
DI unsigned umax(unsigned a, unsigned b) { return a > b ? a : b; }
DI unsigned nosentf(float f) { const unsigned u = __float_as_uint(f); return u < 0xFFFFFFFEu ? u : 0xFFFFFFFEu; }
DI unsigned mx4(v4u v) { return umax(umax(v.x, v.y), umax(v.z, v.w)); }
DI bool none(bool p) { return __builtin_amdgcn_ballot_w64(p) == 0ull; }
#define CBAR() asm volatile("" ::: "memory")
#define LGKM0() asm volatile("s_waitcnt lgkmcnt(0)" ::: "memory")
#define SB() __builtin_amdgcn_sched_barrier(0)
#define PIN(x) asm volatile("" : "+v"(x))
#define F4(v) __builtin_bit_cast(f32x4, v)
#define U2F(u) __uint_as_float(u)
#define F2U(f) __float_as_uint(f)
template <int CTRL> DI float dpp(float x) { return __builtin_bit_cast(float, __builtin_amdgcn_mov_dpp(__builtin_bit_cast(int, x), CTRL, 0xf, 0xf, true)); }
constexpr int XOR1 = 0xB1, XOR2 = 0x4E, XOR7 = 0x141, ROR8 = 0x128, ROR4 = 0x124, BCAST = 0x150;
DI float sw16(float a, float b) { auto s = __builtin_amdgcn_permlane16_swap(F2U(a), F2U(b), false, false); return U2F(s[0]) + U2F(s[1]); }
DI float sw32(float a, float b) { auto s = __builtin_amdgcn_permlane32_swap(F2U(a), F2U(b), false, false); return U2F(s[0]) + U2F(s[1]); }
DI float x16sum(float x) { return sw16(x, x); }
DI float x32sum(float x) { return sw32(x, x); }
DI float x16max(float x) { auto s = __builtin_amdgcn_permlane16_swap(F2U(x), F2U(x), false, false); return fmaxf(U2F(s[0]), U2F(s[1])); }
DI float x32max(float x) { auto s = __builtin_amdgcn_permlane32_swap(F2U(x), F2U(x), false, false); return fmaxf(U2F(s[0]), U2F(s[1])); }
DI float rsum(float x) { x += dpp<XOR1>(x); x += dpp<XOR2>(x); x += dpp<XOR7>(x); x += dpp<ROR8>(x); return x; }
DI float wsum(float x) { return x32sum(x16sum(rsum(x))); }
DI float wmax(float x) { x = fmaxf(x, dpp<XOR1>(x)); x = fmaxf(x, dpp<XOR2>(x)); x = fmaxf(x, dpp<XOR7>(x)); x = fmaxf(x, dpp<ROR8>(x)); return x32max(x16max(x)); }
typedef __amdgpu_buffer_rsrc_t rsrc_t;
DI rsrc_t mkrs(const void* p, int bytes) { return __builtin_amdgcn_make_buffer_rsrc((void*)p, 0, bytes, 0x00020000); }
#define LD16(r, vo, so, aux) __builtin_amdgcn_raw_buffer_load_b128((r), (vo), (so), (aux))
#define LD8(r, vo, so, aux) __builtin_amdgcn_raw_buffer_load_b64((r), (vo), (so), (aux))
#define LD4(r, vo, so, aux) __builtin_amdgcn_raw_buffer_load_b32((r), (vo), (so), (aux))
#define ST16(v, r, vo, so, aux) do { __builtin_amdgcn_raw_buffer_store_b128((v), (r), (vo), (so), (aux)); __builtin_amdgcn_sched_barrier(0); asm volatile("s_nop 1" ::: "memory"); __builtin_amdgcn_sched_barrier(0); } while (0)
#define ST8(v, r, vo, so, aux) __builtin_amdgcn_raw_buffer_store_b64((v), (r), (vo), (so), (aux))
#define ST4(v, r, vo, so, aux) __builtin_amdgcn_raw_buffer_store_b32((v), (r), (vo), (so), (aux))
constexpr int NT = 2, SC1 = 16;
DI void lds_wait(volatile LAS unsigned* f, unsigned want) {
#pragma nounroll
  for (int i = 0; i < SPIN * 8; ++i) { if (__builtin_amdgcn_readfirstlane(*f) >= want) break; __builtin_amdgcn_s_sleep(1); }
  CBAR();
}
DI void lds_arrive(LAS unsigned* c, int lane) {
  LGKM0();
  if (lane == 0) __hip_atomic_fetch_add(c, 1u, __ATOMIC_RELAXED, __HIP_MEMORY_SCOPE_WORKGROUP);
  CBAR();
}
#define BARRIER() do { LGKM0(); __builtin_amdgcn_s_barrier(); CBAR(); } while (0)
#define VMW(n) asm volatile("s_waitcnt vmcnt(%0)" :: "n"(n) : "memory")
DI v4u selfrag(unsigned p, const v4u& mk) { return v4u{p & mk.x, p & mk.y, p & mk.z, p & mk.w}; }
DI v4u selmask(int c) { const int r7 = c & 7; const unsigned hm = (r7 & 1) ? 0xffff0000u : 0x0000ffffu;
  return v4u{(r7 >> 1) == 0 ? hm : 0u, (r7 >> 1) == 1 ? hm : 0u, (r7 >> 1) == 2 ? hm : 0u, (r7 >> 1) == 3 ? hm : 0u}; }
DI unsigned permlo(unsigned a, unsigned b) { return __builtin_amdgcn_perm(b, a, 0x05040100u); }
DI unsigned permhi(unsigned a, unsigned b) { return __builtin_amdgcn_perm(b, a, 0x07060302u); }
template <int N> DI unsigned badtags(const v4u (&u)[N], unsigned elo, unsigned ehi) {
  unsigned bad = 0;
#pragma unroll
  for (int k = 0; k < N; ++k) bad |= (u[k].y ^ elo) | (u[k].w ^ ehi);
  return bad;
}

constexpr int D = 640, HQ = 32, SK = 16384, DH = 128, WIN = 4096, DFF = 1728;
#define KV_WIN 8
#define PV_AT 31
#define T_POLLB 1000
#define CODEPF2 0
#define L2PF 1
#define PFAUX NT
#define SV_WQ 6
#define SV_M2 2
#define ABL 0
#define SV_Q1 2
#define STAMP 0
constexpr int WS_EPOCH = 0;
constexpr int WS_MP = 4096;
constexpr int WS_QF = WS_MP + 61440;
constexpr int WS_FL = WS_QF + 16384;
constexpr int WS_OP = WS_FL + 8192;
constexpr int WS_YP = WS_OP + 122880;
constexpr int WS_FP = WS_YP + 20480;
constexpr int WS_BYTES = WS_FP + 69120;
constexpr int WS_STAMP = 1 << 20;
constexpr int L_K = 0;
constexpr int L_P = 147456;
constexpr int L_Q = L_P + 6656;
constexpr int L_OM = L_Q + 256;
constexpr int L_F = L_OM + 4096;
constexpr int L_END = L_F + 256;
enum { F_KA = 0  , F_KB = 4  , F_FREE = 8  , F_QRDY = 12, F_CML = 13, F_CP = 14, F_HRDY = 15, F_CQ = 26, F_QPUB = 27, F_M = 16  , F_L = 20  , F_MCU = 24, F_LCU = 25 };
constexpr int S_W1 = 0;
constexpr int S_W2 = 81920;
constexpr int S_MB = 131072;
constexpr int S_S = S_MB + 4096;
constexpr int S_Y = S_S + 2560;
constexpr int S_Z = S_Y + 2048;
constexpr int S_X1 = S_Z + 2560;
constexpr int S_H = S_X1 + 1280;
constexpr int S_MV = S_H + 128;
constexpr int S_HT = S_MV + 7680;
constexpr int S_QM = 0;
constexpr int S_ST = S_HT + 2560;
enum { G_M1 = 0, G_M2 = 1, G_W1 = 2, G_W2 = 3, G_CV = 4, G_P1 = 5, G_P2 = 6, G_P3 = 7, G_Y = 8, G_H = 9, G_ARM = 10, G_Q = 11 };
static_assert(S_ST + 64 <= L_F && L_END <= 163840, "");

#define TS(k) do {} while (0)
#define TS_FLUSH() do {} while (0)
#define FOLD4(d_) (((d_).x + dpp<0x104>((d_).y)) + (dpp<0x108>((d_).z) + dpp<0x10C>((d_).w)))
DI float dot2(unsigned a, unsigned b, float acc) { return __builtin_amdgcn_fdot2_f32_bf16(__builtin_bit_cast(bf16x2, a), __builtin_bit_cast(bf16x2, b), acc, false); }
#define DMA(m0_, vo_, srd_, so_) do { unsigned keep_; \
    asm volatile("s_mov_b32 %0, m0\n\ts_mov_b32 m0, %1\n\ts_nop 0\n\tbuffer_load_dwordx4 %2, %3, %4 offen nt lds\n\ts_mov_b32 m0, %0" \
                 : "=&s"(keep_) : "s"(m0_), "v"(vo_), "s"(srd_), "s"(so_) : "memory"); } while (0)

DI v2u ld_epoch(rsrc_t rws, int lane) { return LD8(rws, lane == 0 ? 0 : OOB, WS_EPOCH, SC1); }
#define EPOCH_TAG(ep_, lo_, hi_) do { const unsigned long long e_ = (((unsigned long long)__builtin_amdgcn_readfirstlane((ep_).y) << 32) | (unsigned)__builtin_amdgcn_readfirstlane((ep_).x)) + 1ull; lo_ = (unsigned)e_; hi_ = (unsigned)(e_ >> 32); } while (0)
__global__ void __launch_bounds__(512, 2) fused_kernel(const u16* kc, const u16* vc, char* ws, const u16* wmod, const u16* cvec, const u16* x,
    const u16* wq, const u16* wo, const u16* w1, const u16* w2, u16* out) {
  __shared__ __attribute__((aligned(16))) char smem[L_END];
  LAS char* L = (LAS char*)smem;
  const int tid = threadIdx.x, lane = tid & 63, wave = __builtin_amdgcn_readfirstlane(tid >> 6);
  const int b = blockIdx.x, xg = b & 7, r = b >> 3;
  const int g = lane >> 4, c = lane & 15;
  TS(0);
  const unsigned lds0 = (unsigned)(uintptr_t)L;
  const unsigned long long tstart = __builtin_amdgcn_s_memrealtime();
  unsigned long long pc0; unsigned pclen;
  asm volatile("s_getpc_b64 %0\n.Lpf_here:\n\ts_mov_b32 %1, .Lpf_end-.Lpf_here\n\ts_max_i32 %1, %1, .Lpf_end2-.Lpf_here" : "=s"(pc0), "=s"(pclen) :: "scc");

  if (__builtin_expect(r < 24, 1)) {
    volatile LAS unsigned* FL = (volatile LAS unsigned*)(L + L_F);
    const int hd = xg * 4 + (r & 3), sp = r >> 2, j = wave & 3;
    const int nr = sp < 4 ? 43 : 42, r0 = sp * 43 - (sp == 5 ? 1 : 0);
    const size_t kvoff = ((size_t)hd * SK + (SK - WIN) + r0 * 16) * DH;
    if (__builtin_expect(wave < 4, 1)) {
      const unsigned long long ka = (unsigned long long)(kc + kvoff);
      const v4u srdK = {(unsigned)ka, (unsigned)(ka >> 32), (unsigned)nr * 4096u, 0x00020000u};
      int vo[4];
#pragma unroll
      for (int q = 0; q < 4; ++q) vo[q] = g * 256 + ((c ^ (4 * q + g)) * 16);
      const unsigned lbase = lds0 + L_K + j * 36864;
      asm volatile("s_nop 4" ::: "memory");
#pragma nounroll
      for (int i4 = 0; i4 < 11; ++i4) {
        if (i4 == 9) lds_wait(FL + F_FREE + j, 1);
        const unsigned mb = lbase + (i4 < 9 ? i4 : i4 - 9) * 4096, sb = i4 * 16384 + j * 1024;
#pragma unroll
        for (int q = 0; q < 4; ++q) { DMA(mb + q * 1024, vo[q], srdK, sb + q * 4096); if (q == 2 && i4 == 0) __builtin_amdgcn_s_barrier();   if (i4 < KV_N1) VMW(KV_W1); else VMW(KV_WIN); }
        if (i4 == (8 + KV_WIN + 3) / 4 - 1) FL[F_KA + j] = 1u;
      }
      TS(1);
      const rsrc_t rws = mkrs(ws, WS_BYTES);
      const rsrc_t rv = mkrs(vc + kvoff, nr * 4096);
      const v2u ep = ld_epoch(rws, lane);
      v4u V[43];
#pragma unroll
      for (int i = 0; i <= PV_AT; ++i) {
        V[i] = LD16(rv, lane * 16, i * 4096 + j * 1024, NT);
        VMW(KV_WIN);
        if (i == KV_WIN - 1) { FL[F_KB + j] = 1u; TS(2); }
        SB();
      }
      unsigned elo, ehi;
      EPOCH_TAG(ep, elo, ehi);
      const v4u mk = selmask(c);
      int sto = lane < 32 ? c * 32 + g * 16 : (lane == 32 ? 512 : OOB);
      PIN(sto);
      TS(3);
      lds_wait(FL + F_CP, 4);
      TS(4);
      const LAS v4u* pt = (const LAS v4u*)(L + L_P + (((j * 2 + (c >> 3)) * 4 + g) * 52) * 4);
      v4u p4[11];
#pragma unroll
      for (int i4 = 0; i4 < 11; ++i4) p4[i4] = pt[i4];
      unsigned mcu = FL[F_MCU], lcu = FL[F_LCU];
      SB();
      f32x4 acc = {0.f, 0.f, 0.f, 0.f};
      constexpr int PER = (43 - KV_WIN - 1) / (42 - PV_AT);
#pragma unroll
      for (int i = PV_AT + 1; i < 43; ++i) {
        V[i] = LD16(rv, lane * 16, i * 4096 + j * 1024, NT);
        SB();
#pragma unroll
        for (int u = 0; u < PER; ++u) { const int n = (i - PV_AT - 1) * PER + u; acc = mfma(selfrag(p4[n >> 2][n & 3], mk), V[n], acc); }
        SB();
        VMW(KV_WIN);
        SB();
      }
      PIN(mcu); PIN(lcu);
#pragma unroll
      for (int n = (42 - PV_AT) * PER; n < 43; ++n) acc = mfma(selfrag(p4[n >> 2][n & 3], mk), V[n], acc);
      ((LAS f32x4*)(L + L_OM))[j * 64 + lane] = acc;
      TS(5);
      BARRIER();
      if (j == 0) {
        const LAS f32x4* om = (const LAS f32x4*)(L + L_OM) + (lane & 31);
        const f32x4 o = ((om[0] + om[32]) + (om[64] + om[96])) + ((om[128] + om[160]) + (om[192] + om[224]));
        const bool ml = lane >= 32;
        const v4u st = {ml ? mcu : cvtpk(o.x, o.y), elo, ml ? lcu : cvtpk(o.z, o.w), ehi};
        ST16(st, rws, sto, WS_OP + (hd * 6 + sp) * 640, SC1);
        TS(6);
      }
      TS_FLUSH();
      return;
    }
    const rsrc_t rws = mkrs(ws, WS_BYTES);
    unsigned pf[10], pfc = 0;
    if (r < CODEPF) pfc = LD4(mkrs((const void*)pc0, (int)pclen), lane * 128, j * 8192, 0);
    {
      const int t = r * 4 + j;
      const rsrc_t rm = mkrs(wmod, D * 6 * D * 2), rq = mkrs(wq, D * HQ * DH * 2);
#pragma unroll
      for (int i = 0; i < 3; ++i) { const int n = t + 96 * i, um = xg * 5 + n / 40;
        pf[i] = LD4(rm, n < 200 ? (lane >> 3) * 7680 + (lane & 7) * 16 : OOB, ((um & 1) * 320 + 8 * (n % 40)) * 7680 + (um >> 1) * 128, PFAUX); }
#pragma unroll
      for (int i = 0; i < 7; ++i) pf[3 + i] = LD4(rq, lane * 16, (t + 96 * i) * 8192 + xg * 1024, PFAUX);
      SB(); CBAR();
    }
    v2u ep = ld_epoch(rws, lane);
    FL[lane] = 0u;
    BARRIER();
    const LAS char* kb = L + L_K + j * 36864 + c * 256;
    v4u kf[11][4];
    lds_wait(FL + F_KA + j, 1);
#pragma unroll
    for (int t = 0; t < 2; ++t)
#pragma unroll
      for (int ks = 0; ks < 4; ++ks) kf[t][ks] = *(const LAS v4u*)(kb + t * 4096 + (((4 * ks + g) ^ c) * 16));
    SB();
    LGKM0();
    FL[F_FREE + j] = 1u;
#pragma unroll
    for (int i = 0; i < 10; ++i) asm volatile("" :: "v"(pf[i]));
    asm volatile("" :: "v"(pfc));
    TS(1);
    if (j == 0) {
      unsigned elo, ehi;
      EPOCH_TAG(ep, elo, ehi);
#pragma nounroll
      for (int it = 0; it < SPIN * 4; ++it) { if (__builtin_amdgcn_s_memrealtime() - tstart >= T_POLLQ) break; __builtin_amdgcn_s_sleep(2); }
      v4u qu[1];
      bool bd = lane < 32;
#pragma nounroll
      for (int it = 0; it < SPIN; ++it) {
        CBAR();
        if (bd) { qu[0] = LD16(rws, lane * 16, WS_QF + hd * 512, SC1); bd = badtags<1>(qu, elo, ehi) != 0u; }
        if (none(bd)) break;
      }
      if (lane < 32) ((LAS v2u*)(L + L_Q))[lane] = v2u{qu[0].x, qu[0].z};
      LGKM0();
      FL[F_QRDY] = 1u;
      TS(5);
    }
    TS(2);
    lds_wait(FL + F_KB + j, 1);
#pragma unroll
    for (int t = 2; t < 11; ++t)
#pragma unroll
      for (int ks = 0; ks < 4; ++ks) kf[t][ks] = *(const LAS v4u*)(kb + (t < 9 ? t : t - 9) * 4096 + (((4 * ks + g) ^ c) * 16));
    SB();
    lds_wait(FL + F_QRDY, 1);
    TS(3);
    v4u qa[4];
#pragma unroll
    for (int ks = 0; ks < 4; ++ks) qa[ks] = *(const LAS v4u*)(L + L_Q + (4 * ks + g) * 16);
    float sc[11];
#pragma unroll
    for (int t = 0; t < 11; ++t) {
      f32x4 d = {0.f, 0.f, 0.f, 0.f};
#pragma unroll
      for (int ks = 0; ks < 4; ++ks) d = mfma(qa[ks], kf[t][ks], d);
      sc[t] = d.x;
    }
#pragma unroll
    for (int t = 0; t < 11; ++t) PIN(sc[t]);
    const bool g1 = g & 1, g2 = g & 2;
    float s0 = g2 ? (g1 ? sc[3] : sc[2]) : (g1 ? sc[1] : sc[0]);
    float s1 = g2 ? (g1 ? sc[7] : sc[6]) : (g1 ? sc[5] : sc[4]);
    float s2 = g2 ? sc[10] : (g1 ? sc[9] : sc[8]);
    s0 = rbf(rbf(s0) * 0.08838834764831845f); s1 = rbf(rbf(s1) * 0.08838834764831845f); s2 = rbf(rbf(s2) * 0.08838834764831845f);
    const bool v2 = 128 + lane < 4 * nr;
    if (!v2) s2 = -INFINITY;
    const float m = wmax(fmaxf(fmaxf(s0, s1), s2));
    float p0 = __expf(s0 - m), p1 = __expf(s1 - m), p2 = v2 ? __expf(s2 - m) : 0.f;
    const float l = wsum((p0 + p1) + p2);
    if (lane == 0) { FL[F_M + j] = F2U(m); FL[F_L + j] = F2U(l); }
    lds_arrive((LAS unsigned*)(L + L_F) + F_CML, lane);
    lds_wait(FL + F_CML, 4);
    {
      const f32x4 m4 = *(const LAS f32x4*)(L + L_F + F_M * 4), l4 = *(const LAS f32x4*)(L + L_F + F_L * 4);
      const float mcu = fmaxf(fmaxf(m4.x, m4.y), fmaxf(m4.z, m4.w));
      const float lcu = (l4.x * __expf(m4.x - mcu) + l4.y * __expf(m4.y - mcu)) + (l4.z * __expf(m4.z - mcu) + l4.w * __expf(m4.w - mcu));
      const float f = __expf(m - mcu) / lcu;
      p0 *= f; p1 *= f; p2 *= f;
      if (lane == 0 && j == 0) { FL[F_MCU] = F2U(mcu); FL[F_LCU] = F2U(lcu); }
    }
    const unsigned h0 = cvtpk(p0, p0), h1 = cvtpk(p1, p1), h2 = cvtpk(p2, p2);
    const float e0 = p0 - bflo(h0), e1 = p1 - bflo(h1), e2 = p2 - bflo(h2);
    LAS unsigned* pw = (LAS unsigned*)(L + L_P) + (j * 8 + (lane & 3)) * 52 + (lane >> 2);
    pw[0] = h0; pw[16] = h1; pw[32] = h2; pw[208] = cvtpk(e0, e0); pw[224] = cvtpk(e1, e1); pw[240] = cvtpk(e2, e2);
    lds_arrive((LAS unsigned*)(L + L_F) + F_CP, lane);
    TS(4);
    TS_FLUSH();
    return;
  }

  const int a = wave & 3, k = r - 24;
  const bool role_b = k < 5, role_c = k >= 5 || (k == 0 && xg < 3), role_m = !(k == 0 && xg < 4);
  const int uc = k >= 5 ? xg * 3 + k - 5 : 24 + xg;
  const int chi = c & 7, rho = c >> 3;
  const rsrc_t rws = mkrs(ws, WS_BYTES);
  const rsrc_t rx = mkrs(x, D * 2);
  volatile LAS unsigned* FL = (volatile LAS unsigned*)(L + L_F);
  LAS unsigned* FC = (LAS unsigned*)(L + L_F);
  auto red4 = [&](f32x4 t) { return sw32(sw16(t.x, t.y), sw16(t.z, t.w)); };
  unsigned elo, ehi;
  const bool role_q = k >= 1 && k <= 4;

  if (wave < 4) {
    const int hb = xg * 4 + a;
    const v2u ep = ld_epoch(rws, lane);
    __builtin_amdgcn_s_barrier();
    EPOCH_TAG(ep, elo, ehi);
    if (role_q) {
      v4u WQ[40];
      const rsrc_t rq = mkrs(wq, D * HQ * DH * 2);
#pragma unroll
      for (int i = 0; i < 40; ++i) { WQ[i] = LD16(rq, (40 * g) * 8192 + c * 16, (160 * a + i) * 8192 + (xg * 4 + k - 1) * 256, NT); VMW(SV_WQ); SB(); }
      TS(3);
      const v4u mk = selmask(c);
      lds_wait(FL + G_H, 4);
      const LAS v4u* ht = (const LAS v4u*)(L + S_HT + (160 * a + 40 * g) * 4);
      f32x4 acc = {0.f, 0.f, 0.f, 0.f}, acc2 = {0.f, 0.f, 0.f, 0.f};
      v4u h4[10];
#pragma unroll
      for (int i = 0; i < 10; ++i) h4[i] = ht[i];
      SB();
#pragma unroll
      for (int i = 0; i < 40; i += 2) { acc = mfma(selfrag(h4[i >> 2][i & 3], mk), WQ[i], acc); acc2 = mfma(selfrag(h4[i >> 2][(i & 3) + 1], mk), WQ[i + 1], acc2); }
      if (lane < 32) ((LAS f32x4*)(L + S_QM))[a * 32 + c * 2 + g] = acc + acc2;
      lds_arrive(FC + G_Q, lane);
      lds_wait(FL + G_Q, 4);
      if (a == 0) {
        const LAS f32x2* mb = (const LAS f32x2*)(L + S_QM) + lane;
        const f32x2 qv = (mb[0] + mb[64]) + (mb[128] + mb[192]);
        const unsigned qp = cvtpk(qv.x, qv.y), qn = F2U(dpp<XOR1>(U2F(qp)));
        ST16((v4u{qp, elo, qn, ehi}), rws, !(lane & 1) ? lane * 8 : OOB, WS_QF + (xg * 4 + k - 1) * 512, SC1);
      } else __builtin_amdgcn_s_sleep(6);
      TS(4);
    } else if (k == 5) lds_wait(FL + G_M1, 5);
    v4u WB[32];
    if (role_b) {
      const rsrc_t ro = mkrs(wo, HQ * DH * D * 2);
#pragma unroll
      for (int i = 0; i < 32; ++i) {
        WB[i] = LD16(ro, (4 * g) * 1280 + c * 16, (hb * 128 + 64 * (i >> 4) + 32 * ((i >> 2) & 1) + 16 * ((i >> 3) & 1) + (i & 3)) * 1280 + k * 256, NT);
        VMW(SV_W); SB();
      }
    }
    TS(1);
    if (role_c) {
      const unsigned long long wa = (unsigned long long)w1, wb = (unsigned long long)(w2 + (size_t)uc * 64 * D);
      const v4u srd1 = {(unsigned)wa, (unsigned)(wa >> 32), (unsigned)(D * DFF * 2), 0x00020000u};
      const v4u srd2 = {(unsigned)wb, (unsigned)(wb >> 32), 81920u, 0x00020000u};
      const int f0 = (g & 1) | ((a & 1) << 2);
      const int voa = (lane >> 3) * 3456 + (((lane & 7) ^ f0) * 16), vob = (lane >> 3) * 3456 + (((lane & 7) ^ f0 ^ 2) * 16);
      asm volatile("s_nop 4" ::: "memory");
#pragma nounroll
      for (int i = 0; i < 20; i += 2) {
        const unsigned m0v = lds0 + S_W1 + (a * 20 + i) * 1024, so = (a * 160 + i * 8) * 3456 + uc * 128;
        DMA(m0v, voa, srd1, so); VMW(SV_W); DMA(m0v + 1024, vob, srd1, so + 8 * 3456); VMW(SV_W);
      }
#pragma nounroll
      for (int i = 0; i < 20; ++i) {
        const int p = 4 * i + a, row = ((4 * p + g) * 205) >> 10;
        const int vo2 = g * 256 + ((c ^ (((row & 3) | ((row >> 1) & 4)) << 1)) * 16);
        if (i == 12) lds_wait(FL + G_CV, 4);
        const unsigned m0v = lds0 + (i < 12 ? S_W2 + p * 1024 : S_W1 + (p - 48) * 1024), so = p * 1024;
        DMA(m0v, vo2, srd2, so); VMW(SV_W);
        if (i == SV_W) lds_arrive(FC + G_W1, lane);
      }
      VMW(0);
      lds_arrive(FC + G_W2, lane);
    }
    TS(2);
    if (!role_b) { TS_FLUSH(); return; }
    v4u BF[4][8];
#pragma unroll
    for (int hf = 0; hf < 4; ++hf) {
#pragma unroll
      for (int m = 0; m < 4; ++m)
#pragma unroll
        for (int d = 0; d < 4; ++d) { BF[hf][2 * d][m] = permlo(WB[hf * 8 + 2 * m][d], WB[hf * 8 + 2 * m + 1][d]); BF[hf][2 * d + 1][m] = permhi(WB[hf * 8 + 2 * m][d], WB[hf * 8 + 2 * m + 1][d]); }
#pragma unroll
      for (int t = 0; t < 8; ++t) PIN(BF[hf][t]);
    }
    const bool role_d = (a == 1 && k > 0) || (a == 2 && k == 1);
    const int v = xg * 5 + (a == 1 ? k : 0);
    const int q4 = lane & 3, part = lane >> 2;
    float dx = 0.f, dga = 0.f, dgb = 0.f;
    if (role_d) {
      const unsigned xv = LD4(rx, q4 * 8 + (g >> 1) * 4, v * 32, 0);
      v4u mm[2];
      bool bd = true;
#pragma nounroll
      for (int it = 0; it < SPIN; ++it) {
        CBAR();
        if (bd) {
          mm[0] = LD16(rws, ((lane >> 3) & 1) * 30720 + ((lane >> 4) & 1) * 15360 + (lane & 7) * 16, WS_MP + 10240 + v * 128, SC1);
          mm[1] = LD16(rws, lane * 128, WS_FL, SC1);
          bd = badtags<2>(mm, elo, ehi) != 0u;
        }
        if (none(bd)) break;
        __builtin_amdgcn_s_sleep(16);
      }
      const bool odd = g & 1;
      dx = odd ? bfhi(xv) : bflo(xv);
      const float e0 = U2F(mm[0].x) + dpp<ROR8>(U2F(mm[0].x)), e1 = U2F(mm[0].z) + dpp<ROR8>(U2F(mm[0].z));
      const int src = q4 * 2 + (g >> 1);
      const float a0 = __shfl(e0, src), a1 = __shfl(e1, src), b0 = __shfl(e0, 16 + src), b1 = __shfl(e1, 16 + src);
      dga = rbf(odd ? a1 : a0); dgb = rbf(odd ? b1 : b0);
      PIN(dx); PIN(dga); PIN(dgb);
    }
    lds_wait(FL + G_ARM, 1);
    TS(5);
#pragma nounroll
    for (int it = 0; it < SPIN * 4; ++it) { if (__builtin_amdgcn_s_memrealtime() - tstart >= T_POLLB) break; __builtin_amdgcn_s_sleep(4); }
    {
      const int s2 = (c + 4 * g) & 7;
      int vo1 = (c & 7) * 640 + (c >> 3) * 64 + g * 16, vo2 = s2 * 640 + 512;
      unsigned m_lo = c < 6 ? ~0u : 0u, m_hi = (c >= 8 && c < 14) ? ~0u : 0u;
      int lw = S_Y + (a * 128 + c * 8 + g) * 4;
      PIN(vo1); PIN(vo2); PIN(lw); PIN(m_lo); PIN(m_hi);
      v4u oa[4], ml[1];
      ml[0] = v4u{0xff800000u, elo, 0u, ehi};
#pragma unroll
      for (int u = 0; u < 4; ++u) oa[u] = v4u{0u, elo, 0u, ehi};
      bool b1 = (c & 7) < 6, b2 = s2 < 6;
#pragma nounroll
      for (int it = 0; it < SPIN; ++it) {
        CBAR();
        if (b2) ml[0] = LD16(rws, vo2, WS_OP + hb * 3840, SC1);
        if (b1) {
#pragma unroll
          for (int u = 0; u < 4; ++u) oa[u] = LD16(rws, vo1, WS_OP + hb * 3840 + u * 128, SC1);
        }
        b2 = badtags<1>(ml, elo, ehi) != 0u; b1 = badtags<4>(oa, elo, ehi) != 0u;
        if (none(b1 | b2)) break;
      }
      TS(6);
      f32x4 d[8];
#pragma unroll
      for (int t = 0; t < 8; ++t) d[t] = f32x4{0.f, 0.f, 0.f, 0.f};
#pragma unroll
      for (int hf = 0; hf < 4; ++hf) {
        const unsigned m_ = (hf & 1) ? m_hi : m_lo;
        const v4u af = {oa[hf & 2].x & m_, oa[hf & 2].z & m_, oa[(hf & 2) + 1].x & m_, oa[(hf & 2) + 1].z & m_};
#pragma unroll
        for (int t = 0; t < 8; ++t) d[t] = mfma(af, BF[hf][t], d[t]);
      }
      const float ms = U2F(ml[0].x);
      float M = fmaxf(ms, dpp<XOR1>(ms)); M = fmaxf(M, dpp<XOR2>(M)); M = fmaxf(M, dpp<XOR7>(M));
      const float e = U2F(ml[0].z) * __expf(ms - M);
      float Ls = e + dpp<XOR1>(e); Ls += dpp<XOR2>(Ls); Ls += dpp<XOR7>(Ls);
      const float wt = e * __builtin_amdgcn_rcpf(Ls);
      const float w0 = dpp<BCAST + 0>(wt), w1_ = dpp<BCAST + 1>(wt), w2_ = dpp<BCAST + 2>(wt), w3 = dpp<BCAST + 3>(wt);
      float y[8];
#pragma unroll
      for (int t = 0; t < 8; ++t) y[t] = fmaf(d[t].w, w3, fmaf(d[t].z, w2_, fmaf(d[t].y, w1_, d[t].x * w0)));
      const float ya = sw32(sw16(y[0], y[1]), sw16(y[2], y[3])), yb = sw32(sw16(y[4], y[5]), sw16(y[6], y[7]));
      *(LAS float*)(L + lw) = ya; *(LAS float*)(L + lw + 16) = yb;
      unsigned old = 0;
      LGKM0();
      if (lane == 0) old = __hip_atomic_fetch_add(FC + G_Y, 1u, __ATOMIC_RELAXED, __HIP_MEMORY_SCOPE_WORKGROUP);
      if (__builtin_amdgcn_readfirstlane(old) == 3) {
        CBAR();
        const LAS f32x2* tp = (const LAS f32x2*)(L + S_Y) + lane;
        const f32x2 yy = (tp[0] + tp[64]) + (tp[128] + tp[192]);
        ST8((v2u{nosentf(yy.x), nosentf(yy.y)}), rws, lane * 8, WS_YP + xg * 2560 + k * 512, SC1);
        TS(7);
      }
    }
    if (role_d) {
      v4u yq;
#pragma nounroll
      for (int it = 0; it < SPIN; ++it) {
        CBAR();
        yq = LD16(rws, q4 * 16 + (part & 7) * 2560, WS_YP + v * 64, SC1);
        if (none(mx4(yq) == SENT)) break;
        __builtin_amdgcn_s_sleep(2);
      }
      auto red = [&](f32x4 t) { float s = red4(t); s += dpp<ROR4>(s); s += dpp<ROR8>(s); return s; };
      float x1 = rbf(dx + rbf(dga * rbf(0.5f * red(F4(yq)))));
      int o0 = q4 * 16 + part * 2560;
      PIN(x1); PIN(o0);
      v4u f0, f1 = {0u, 0u, 0u, 0u};
      bool b0 = true, b1 = part < 11;
#pragma nounroll
      for (int it = 0; it < SPIN; ++it) {
        CBAR();
        if (b0) f0 = LD16(rws, o0, WS_FP + v * 64, SC1);
        if (b1) f1 = LD16(rws, o0, WS_FP + v * 64 + 16 * 2560, SC1);
        b0 = mx4(f0) == SENT; b1 = mx4(f1) == SENT;
        if (none(b0 | b1)) break;
      }
      TS(10);
      const float mo = red(F4(f0) + F4(f1));
      const float res = x1 + rbf(dgb * rbf(mo));
      if (c < 4) ((__bf16*)out)[v * 16 + q4 * 4 + g] = (__bf16)res;
      if (v == 0 && lane == 0) ST8((v2u{elo, ehi}), rws, 0, WS_EPOCH, SC1);
      TS(11);
      asm volatile(".Lpf_end2:");
    }
    TS_FLUSH();
    return;
  }

  {
    const int u1 = (k >= 1 && k <= 5) ? xg * 5 + k - 1 : (k == 0 ? 72 + xg * 2 : 48 + xg * 4 + (k - 6) * 2);
    const int u2 = role_q ? 88 + xg * 4 + k - 1 : (k == 5 ? 40 + xg : u1 + 1);
    const rsrc_t rm = mkrs(wmod, D * 6 * D * 2);
    const int mvo = (10 * (2 * g + rho)) * 7680 + chi * 16;
    v4u WM[10];
    const v4u mk = selmask(c);
    auto mod_issue = [&](int um) {
#pragma unroll
      for (int i = 0; i < 10; ++i) WM[i] = LD16(rm, mvo, (320 * (um & 1) + 80 * a + i) * 7680 + (um >> 1) * 128, NT);
      SB(); CBAR();
    };
    auto mod_unit = [&](int u, int cnt, int um) {
      const LAS v2u* sp_ = (const LAS v2u*)(L + S_S) + (um & 1) * 160 + a * 40 + 5 * (2 * g + rho);
      v2u sv2[5];
#pragma unroll
      for (int i = 0; i < 5; ++i) sv2[i] = sp_[i];
      f32x4 acc = {0.f, 0.f, 0.f, 0.f};
#pragma unroll
      for (int i = 0; i < 5; ++i) { acc = mfma(selfrag(sv2[i].x, mk), WM[2 * i], acc); acc = mfma(selfrag(sv2[i].y, mk), WM[2 * i + 1], acc); }
      if ((g >> 1) == rho) *(LAS f32x4*)(L + S_MB + u * 2048 + ((a * 2 + rho) * 16 + chi * 2 + (g & 1)) * 16) = acc;
      lds_arrive(FC + cnt, lane);
      if (a == 0) {
        lds_wait(FL + cnt, 4);
        const LAS f32x2* mb = (const LAS f32x2*)(L + S_MB + u * 2048) + (lane & 31);
        const f32x2 mv = ((mb[0] + mb[32]) + (mb[64] + mb[96])) + ((mb[128] + mb[160]) + (mb[192] + mb[224]));
        ST16((v4u{F2U(mv.x), elo, F2U(mv.y), ehi}), rws, lane < 32 ? lane * 16 : OOB, WS_MP + (um & 1) * 30720 + (um >> 1) * 512, SC1);
        lds_arrive(FC + cnt, lane);
      }
    };
    FL[lane] = 0u;
    v2u ep = ld_epoch(rws, lane);
    unsigned xr[5];
#pragma unroll
    for (int i = 0; i < 5; ++i) xr[i] = LD4(rx, lane * 4, i * 256, 0);
    const unsigned xa = LD4(rx, lane * 4, a * 256, 0), xb = LD4(rx, c * 4, 1024 + a * 64, 0);
    if (role_m) {
      const v2u cv = LD8(mkrs(cvec, D * 2), lane < 40 ? (lane >= 20 ? 640 + (lane - 20) * 8 : lane * 8) : OOB, a * 160, 0);
      mod_issue(u1);
      BARRIER();
      float cf[4] = {bflo(cv.x), bfhi(cv.x), bflo(cv.y), bfhi(cv.y)};
      unsigned sd[4];
#pragma unroll
      for (int i = 0; i < 4; ++i) { const float sv = cf[i] / (1.f + __expf(-cf[i])); sd[i] = cvtpk(sv, sv); }
      if (lane < 40) ((LAS v4u*)(L + S_S))[(lane >= 20 ? 60 + lane : lane) + a * 20] = v4u{sd[0], sd[1], sd[2], sd[3]};
      LGKM0();
      PIN(ep);
      EPOCH_TAG(ep, elo, ehi);
      mod_unit(0, G_M1, u1);
    } else {
      BARRIER();
      PIN(ep);
      EPOCH_TAG(ep, elo, ehi);
    }
    TS(1);
    if (a == 3) {
      const v4u sv = {SENT, SENT, SENT, SENT};
      ST16(sv, rws, (role_b && lane < 32) ? lane * 16 : OOB, WS_YP + xg * 2560 + k * 512, SC1);
      if (role_c) { ST16(sv, rws, lane * 16, WS_FP + uc * 2560, SC1); ST16(sv, rws, lane * 16, WS_FP + uc * 2560 + 1024, SC1); ST16(sv, rws, lane < 32 ? lane * 16 : OOB, WS_FP + uc * 2560 + 2048, SC1); }
    }
    if (role_q) {
      float s = 0.f;
#pragma unroll
      for (int i = 0; i < 5; ++i) s += bflo(xr[i]) + bfhi(xr[i]);
      float mean = wsum(s) * (1.f / D);
      float v = 0.f;
#pragma unroll
      for (int i = 0; i < 5; ++i) { const float d0 = bflo(xr[i]) - mean, d1 = bfhi(xr[i]) - mean; v += d0 * d0 + d1 * d1; }
      v = wsum(v) * (1.f / D);
      const float den = rbf(sqrtf(rbf(rbf(v) + 1e-5f)));
      mean = rbf(mean);
      float n0 = rbf(rbf(bflo(xa) - mean) / den), n1 = rbf(rbf(bfhi(xa) - mean) / den), n2 = rbf(rbf(bflo(xb) - mean) / den), n3 = rbf(rbf(bfhi(xb) - mean) / den);
      PIN(n0); PIN(n1); PIN(n2); PIN(n3);
      v4u u[5];
      bool bd = true;
#pragma nounroll
      for (int it = 0; it < SPIN * 4; ++it) { if (__builtin_amdgcn_s_memrealtime() - tstart >= T_POLL1) break; __builtin_amdgcn_s_sleep(1); }
#pragma nounroll
      for (int it = 0; it < SPIN; ++it) {
        CBAR();
        if (bd) {
#pragma unroll
          for (int i = 0; i < 4; ++i) u[i] = LD16(rws, lane * 16, WS_MP + (i & 1) * 30720 + (i >> 1) * 5120 + a * 1024, SC1);
          u[4] = LD16(rws, (g & 1) * 30720 + (g >> 1) * 5120 + c * 16, WS_MP + 4096 + a * 256, SC1);
          bd = badtags<5>(u, elo, ehi) != 0u;
        }
        if (none(bd)) break;
      }
      const float sa0 = rbf(U2F(u[0].x) + U2F(u[1].x)), sa1 = rbf(U2F(u[0].z) + U2F(u[1].z)), ca0 = rbf(U2F(u[2].x) + U2F(u[3].x)), ca1 = rbf(U2F(u[2].z) + U2F(u[3].z));
      const float h0 = rbf(n0 * rbf(1.f + ca0)) + sa0, h1 = rbf(n1 * rbf(1.f + ca1)) + sa1;
      ((LAS v2u*)(L + S_HT))[a * 64 + lane] = v2u{cvtpk(h0, h0), cvtpk(h1, h1)};
      const float e0 = rbf(x16sum(U2F(u[4].x))), e1 = rbf(x16sum(U2F(u[4].z)));
      auto s0 = __builtin_amdgcn_permlane32_swap(F2U(e0), F2U(e0), false, false); auto s1 = __builtin_amdgcn_permlane32_swap(F2U(e1), F2U(e1), false, false);
      const float h2 = rbf(n2 * rbf(1.f + U2F(s0[1]))) + U2F(s0[0]), h3 = rbf(n3 * rbf(1.f + U2F(s1[1]))) + U2F(s1[0]);
      if (lane < 16) ((LAS v2u*)(L + S_HT))[256 + a * 16 + lane] = v2u{cvtpk(h2, h2), cvtpk(h3, h3)};
      lds_arrive(FC + G_H, lane);
      TS(6);
      lds_wait(FL + G_H, 4);
    }
    if (role_m) { mod_issue(u2); mod_unit(1, G_M2, u2); }
    TS(2);
    if (a == 3) {
      VMW(0);
      ST16((v4u{0u, elo, 0u, ehi}), rws, lane == 0 ? 0 : OOB, WS_FL + (xg * 8 + k) * 128, SC1);
      FL[G_ARM] = 1u;
    }
    TS(3);
  }
  if (!role_c) { TS_FLUSH(); return; }
  typedef short v4s __attribute__((ext_vector_type(4)));
#define TRRD(p_) __builtin_bit_cast(v2u, __builtin_amdgcn_ds_read_tr16_b64_v4i16((LAS v4s*)(p_)))
  v4u B1[5][4];
  v4u B2[2][10];
  const int q_ = c >> 2, p_ = c & 3;
  lds_wait(FL + G_W1, 4);
  {
    const int f_ = (q_ >> 1) | ((g & 1) << 1) | ((p_ & 1) << 2);
    LAS char* wi = L + S_W1 + (160 * p_ + 8 * g + q_) * 128;
#pragma unroll
    for (int t = 0; t < 4; ++t) {
      LAS char* wt = wi + (((2 * a + (t >> 1)) ^ f_) * 16) + (t & 1) * 8;
#pragma unroll
      for (int ks = 0; ks < 5; ++ks) { const v2u lo = TRRD(wt + ks * 4096), hi = TRRD(wt + ks * 4096 + 512); B1[ks][t] = v4u{lo.x, lo.y, hi.x, hi.y}; }
    }
#pragma unroll
    for (int ks = 0; ks < 5; ++ks)
#pragma unroll
      for (int t = 0; t < 4; ++t) PIN(B1[ks][t]);
  }
  lds_arrive(FC + G_CV, lane);
  TS(4);
  const int el = 32 * a + 4 * chi + g;
  float fa[5], fb[5], Cc, Ss;
  const LAS char* zr = L + S_Z + p_ * 320 + g * 16;
  {
    unsigned xe[5];
#pragma unroll
    for (int i = 0; i < 5; ++i) xe[i] = LD4(rx, (el >> 1) * 4, i * 256, 0);
    v4u mm[9];
    const int c3 = a < 3 ? a + 12 : a;
    bool bd = true, bt = lane < 16;
    mm[8] = v4u{0u, elo, 0u, ehi};
#pragma nounroll
    for (int it = 0; it < SPIN; ++it) {
      CBAR();
      if (bd) {
#pragma unroll
        for (int i = 0; i < 8; ++i) mm[i] = LD16(rws, lane * 16, WS_MP + 10240 + (i & 1) * 30720 + (i < 6 ? a + 4 * (i >> 1) : c3) * 1024, SC1);
      }
      if (bt) mm[8] = LD16(rws, lane * 128, WS_FL + a * 2048, SC1);
      bd = badtags<9>(mm, elo, ehi) != 0u; bt = bt && bd;
      if (none(bd)) break;
      __builtin_amdgcn_s_sleep(16);
    }
#pragma unroll
    for (int i = 0; i < 4; ++i) *(LAS f32x2*)(L + S_MV + (i < 3 ? a + 4 * i : c3) * 512 + lane * 8) = f32x2{rbf(U2F(mm[2 * i].x) + U2F(mm[2 * i + 1].x)), rbf(U2F(mm[2 * i].z) + U2F(mm[2 * i + 1].z))};
    lds_arrive(FC + G_P1, lane);
    lds_wait(FL + G_P1, 4);
#pragma unroll
    for (int i = 0; i < 5; ++i) {
      const LAS float* mv = (const LAS float*)(L + S_MV) + el + 128 * i;
      const float gav = mv[0], sbv = mv[640], c1 = rbf(1.f + mv[1280]);
      const float xv = (g & 1) ? bfhi(xe[i]) : bflo(xe[i]);
      fa[i] = rho ? gav : gav * c1; fb[i] = rho ? xv : xv * c1;
      *(LAS __bf16*)(L + S_Z + rho * 1280 + (el + 128 * i) * 2) = (__bf16)(rho ? sbv : c1);
      PIN(fa[i]); PIN(fb[i]);
    }
    lds_arrive(FC + G_P2, lane);
    lds_wait(FL + G_P2, 4);
    f32x4 dc[4], ds[4];
#pragma unroll
    for (int t = 0; t < 4; ++t) { dc[t] = f32x4{0.f, 0.f, 0.f, 0.f}; ds[t] = dc[t]; }
#pragma unroll
    for (int ks = 0; ks < 5; ++ks) {
      const v4u ac = *(const LAS v4u*)(zr + ks * 64), as = *(const LAS v4u*)(zr + 1280 + ks * 64);
#pragma unroll
      for (int t = 0; t < 4; ++t) { dc[t] = mfma(ac, B1[ks][t], dc[t]); ds[t] = mfma(as, B1[ks][t], ds[t]); }
    }
    const bool g1 = g & 1, g2 = g & 2;
#define SEL4(d_) f32x4{g2 ? (g1 ? d_[3].x : d_[2].x) : (g1 ? d_[1].x : d_[0].x), g2 ? (g1 ? d_[3].y : d_[2].y) : (g1 ? d_[1].y : d_[0].y), \
                       g2 ? (g1 ? d_[3].z : d_[2].z) : (g1 ? d_[1].z : d_[0].z), g2 ? (g1 ? d_[3].w : d_[2].w) : (g1 ? d_[1].w : d_[0].w)}
#pragma unroll
    for (int t = 0; t < 4; ++t) { PIN(dc[t]); PIN(ds[t]); }
    { const f32x4 u = SEL4(dc), v = SEL4(ds); Cc = FOLD4(u); Ss = FOLD4(v); }
    PIN(Cc); PIN(Ss);
    lds_arrive(FC + G_P3, lane);
  }
  lds_wait(FL + G_W2, 4);
  {
    const int f2 = (q_ | ((g & 1) << 2)) << 1;
#pragma unroll
    for (int t = 0; t < 10; ++t) {
      const int cb = 320 * a + 32 * t + 8 * p_;
      const int cs = (cb & ~255) | ((((cb >> 4) & 15) ^ f2) << 4) | (cb & 8);
#pragma unroll
      for (int ks = 0; ks < 2; ++ks) {
        v2u h[2];
#pragma unroll
        for (int rd = 0; rd < 2; ++rd) { const int o = (32 * ks + 8 * g + 4 * rd + q_) * 1280 + cs; h[rd] = TRRD(L + (o < 49152 ? S_W2 + o : S_W1 + o - 49152)); }
        B2[ks][t] = v4u{h[0].x, h[0].y, h[1].x, h[1].y};
      }
      PIN(B2[0][t]); PIN(B2[1][t]);
    }
  }
  lds_wait(FL + G_P3, 4);
  TS(5);
  {
    int zw = (rho ? S_X1 : S_Z) + el * 2;
    int hw = c < 4 ? S_H + (a * 16 + 4 * g + c) * 2 : S_ST + 16;
    int yo = (lane >> 3) * 2560 + a * 128 + (lane & 7) * 16;
    int st3 = lane < 32 ? lane * 4 + 512 : OOB;
    PIN(zw); PIN(hw); PIN(yo); PIN(st3);
#pragma nounroll
    for (int it = 0; it < SPIN * 4; ++it) { if (__builtin_amdgcn_s_memrealtime() - tstart >= T_POLLB) break; __builtin_amdgcn_s_sleep(4); }
    v4u yp[5];
    bool bd = true;
#pragma nounroll
    for (int it = 0; it < SPIN; ++it) {
      CBAR();
      if (bd) {
#pragma unroll
        for (int i = 0; i < 5; ++i) yp[i] = LD16(rws, yo, WS_YP + i * 512, SC1);
      }
      bd = umax(umax(umax(mx4(yp[0]), mx4(yp[1])), umax(mx4(yp[2]), mx4(yp[3]))), mx4(yp[4])) == SENT;
      if (none(bd)) break;
    }
    TS(8);
#pragma unroll
    for (int i = 0; i < 5; ++i) {
      float at = red4(F4(yp[i]));
      at += dpp<ROR8>(at);
      *(LAS __bf16*)(L + zw + 256 * i) = (__bf16)fmaf(fa[i], at, fb[i]);
    }
    BARRIER();
    v4u af[5];
#pragma unroll
    for (int ks = 0; ks < 5; ++ks) af[ks] = *(const LAS v4u*)(zr + ks * 64);
    const v4u xa = *(const LAS v4u*)(L + S_X1 + lane * 16);
    const unsigned xb = *(const LAS unsigned*)(L + S_X1 + 1024 + lane * 4);
    SB();
    f32x4 d[4];
#pragma unroll
    for (int t = 0; t < 4; ++t) d[t] = f32x4{0.f, 0.f, 0.f, 0.f};
#pragma unroll
    for (int ks = 0; ks < 5; ++ks)
#pragma unroll
      for (int t = 0; t < 4; ++t) d[t] = mfma(af[ks], B1[ks][t], d[t]);
    const unsigned one2 = 0x3f803f80u;
    const float t1 = dot2(xb, one2, dot2(xa.x, one2, dot2(xa.y, one2, 0.f))) + dot2(xa.z, one2, dot2(xa.w, one2, 0.f));
    const float t2 = dot2(xb, xb, dot2(xa.x, xa.x, dot2(xa.y, xa.y, 0.f))) + dot2(xa.z, xa.z, dot2(xa.w, xa.w, 0.f));
    const float s12 = x32sum(rsum(sw16(t1, t2)));
    const float mean = U2F(__builtin_amdgcn_readlane(F2U(s12), 0)) * (1.f / D);
    const float var = fmaxf(U2F(__builtin_amdgcn_readlane(F2U(s12), 16)) * (1.f / D) - mean * mean, 0.f);
    const float rden = __builtin_amdgcn_rcpf(rbf(__builtin_amdgcn_sqrtf(rbf(rbf(var) + 1e-5f))));
    float tt = Ss - Cc * (rbf(mean) * rden);
    PIN(tt);
    const bool g1 = g & 1, g2 = g & 2;
#pragma unroll
    for (int t = 0; t < 4; ++t) PIN(d[t]);
    const f32x4 dd = SEL4(d);
    *(LAS __bf16*)(L + hw) = (__bf16)fmaxf(fmaf(FOLD4(dd), rden, tt), 0.f);
    BARRIER();
    const v4u ha0 = *(const LAS v4u*)(L + S_H + g * 16), ha1 = *(const LAS v4u*)(L + S_H + 64 + g * 16);
    f32x4 o[10];
#pragma unroll
    for (int t = 0; t < 10; ++t) o[t] = mfma(ha1, B2[1][t], mfma(ha0, B2[0][t], f32x4{0.f, 0.f, 0.f, 0.f}));
    float ov[10];
#pragma unroll
    for (int t = 0; t < 10; ++t) { ov[t] = o[t].x; PIN(ov[t]); }
    ST4(nosentf(g2 ? (g1 ? ov[3] : ov[2]) : (g1 ? ov[1] : ov[0])), rws, lane * 4, WS_FP + uc * 2560 + a * 640, SC1);
    ST4(nosentf(g2 ? (g1 ? ov[7] : ov[6]) : (g1 ? ov[5] : ov[4])), rws, lane * 4, WS_FP + uc * 2560 + a * 640 + 256, SC1);
    ST4(nosentf(g1 ? ov[9] : ov[8]), rws, st3, WS_FP + uc * 2560 + a * 640, SC1);
    TS(9);
  }
  asm volatile(".Lpf_end:");
  TS_FLUSH();
}

extern "C" void kernel_launch(void* const* d_in, const int* in_sizes, int n_in, void* d_out, int out_size, void* d_ws, size_t ws_size, hipStream_t stream) {
  fused_kernel<<<256, 512, 0, stream>>>((const u16*)d_in[1], (const u16*)d_in[2], (char*)d_ws, (const u16*)d_in[9], (const u16*)d_in[8], (const u16*)d_in[0],
      (const u16*)d_in[3], (const u16*)d_in[4], (const u16*)d_in[6], (const u16*)d_in[7], (u16*)d_out);
}
```

```cpp
#include <hip/hip_runtime.h>
#include <hip/hip_bf16.h>
#include <stdint.h>

typedef unsigned short u16;
typedef unsigned v4u __attribute__((ext_vector_type(4)));
typedef unsigned v2u __attribute__((ext_vector_type(2)));
typedef float f32x4 __attribute__((ext_vector_type(4)));
typedef float f32x2 __attribute__((ext_vector_type(2)));
typedef __bf16 bf16x8 __attribute__((ext_vector_type(8)));
typedef __bf16 bf16x2 __attribute__((ext_vector_type(2)));
#define LAS __attribute__((address_space(3)))
#define DI __device__ __forceinline__
constexpr unsigned SENT = 0xFFFFFFFFu;
constexpr int SPIN = 200000;
constexpr int OOB = 0x7fffff00;
DI float bflo(unsigned u) { return __uint_as_float(u << 16); }
DI float bfhi(unsigned u) { return __uint_as_float(u & 0xffff0000u); }
DI unsigned cvtpk(float lo, float hi) { bf16x2 v = {(__bf16)lo, (__bf16)hi}; return __builtin_bit_cast(unsigned, v); }
DI float rbf(float f) { return (float)(__bf16)f; }
DI f32x4 mfma(v4u a, v4u b, f32x4 c) { return __builtin_amdgcn_mfma_f32_16x16x32_bf16(__builtin_bit_cast(bf16x8, a), __builtin_bit_cast(bf16x8, b), c, 0, 0, 0); }
DI unsigned umax(unsigned a, unsigned b) { return a > b ? a : b; }
DI unsigned nosentf(float f) { const unsigned u = __float_as_uint(f); return u < 0xFFFFFFFEu ? u : 0xFFFFFFFEu; }
DI unsigned mx4(v4u v) { return umax(umax(v.x, v.y), umax(v.z, v.w)); }
DI bool none(bool p) { return __builtin_amdgcn_ballot_w64(p) == 0ull; }
#define CBAR() asm volatile("" ::: "memory")
#define LGKM0() asm volatile("s_waitcnt lgkmcnt(0)" ::: "memory")
#define SB() __builtin_amdgcn_sched_barrier(0)
#define PIN(x) asm volatile("" : "+v"(x))
#define F4(v) __builtin_bit_cast(f32x4, v)
#define U2F(u) __uint_as_float(u)
#define F2U(f) __float_as_uint(f)
template <int CTRL> DI float dpp(float x) { return __builtin_bit_cast(float, __builtin_amdgcn_mov_dpp(__builtin_bit_cast(int, x), CTRL, 0xf, 0xf, true)); }
constexpr int XOR1 = 0xB1, XOR2 = 0x4E, XOR7 = 0x141, ROR8 = 0x128, ROR4 = 0x124, BCAST = 0x150;
DI float sw16(float a, float b) { auto s = __builtin_amdgcn_permlane16_swap(F2U(a), F2U(b), false, false); return U2F(s[0]) + U2F(s[1]); }
DI float sw32(float a, float b) { auto s = __builtin_amdgcn_permlane32_swap(F2U(a), F2U(b), false, false); return U2F(s[0]) + U2F(s[1]); }
DI float x16sum(float x) { return sw16(x, x); }
DI float x32sum(float x) { return sw32(x, x); }
DI float x16max(float x) { auto s = __builtin_amdgcn_permlane16_swap(F2U(x), F2U(x), false, false); return fmaxf(U2F(s[0]), U2F(s[1])); }
DI float x32max(float x) { auto s = __builtin_amdgcn_permlane32_swap(F2U(x), F2U(x), false, false); return fmaxf(U2F(s[0]), U2F(s[1])); }
DI float rsum(float x) { x += dpp<XOR1>(x); x += dpp<XOR2>(x); x += dpp<XOR7>(x); x += dpp<ROR8>(x); return x; }
DI float wsum(float x) { return x32sum(x16sum(rsum(x))); }
DI float wmax(float x) { x = fmaxf(x, dpp<XOR1>(x)); x = fmaxf(x, dpp<XOR2>(x)); x = fmaxf(x, dpp<XOR7>(x)); x = fmaxf(x, dpp<ROR8>(x)); return x32max(x16max(x)); }
typedef __amdgpu_buffer_rsrc_t rsrc_t;
DI rsrc_t mkrs(const void* p, int bytes) { return __builtin_amdgcn_make_buffer_rsrc((void*)p, 0, bytes, 0x00020000); }
#define LD16(r, vo, so, aux) __builtin_amdgcn_raw_buffer_load_b128((r), (vo), (so), (aux))
#define LD8(r, vo, so, aux) __builtin_amdgcn_raw_buffer_load_b64((r), (vo), (so), (aux))
#define LD4(r, vo, so, aux) __builtin_amdgcn_raw_buffer_load_b32((r), (vo), (so), (aux))
#define ST16(v, r, vo, so, aux) do { __builtin_amdgcn_raw_buffer_store_b128((v), (r), (vo), (so), (aux)); __builtin_amdgcn_sched_barrier(0); asm volatile("s_nop 1" ::: "memory"); __builtin_amdgcn_sched_barrier(0); } while (0)
#define ST8(v, r, vo, so, aux) __builtin_amdgcn_raw_buffer_store_b64((v), (r), (vo), (so), (aux))
#define ST4(v, r, vo, so, aux) __builtin_amdgcn_raw_buffer_store_b32((v), (r), (vo), (so), (aux))
constexpr int NT = 2, SC1 = 16;
DI void lds_wait(volatile LAS unsigned* f, unsigned want) {
#pragma nounroll
  for (int i = 0; i < SPIN * 8; ++i) { if (__builtin_amdgcn_readfirstlane(*f) >= want) break; __builtin_amdgcn_s_sleep(1); }
  CBAR();
}
DI void lds_arrive(LAS unsigned* c, int lane) {
  LGKM0();
  if (lane == 0) __hip_atomic_fetch_add(c, 1u, __ATOMIC_RELAXED, __HIP_MEMORY_SCOPE_WORKGROUP);
  CBAR();
}
#define BARRIER() do { LGKM0(); __builtin_amdgcn_s_barrier(); CBAR(); } while (0)
#define VMW(n) asm volatile("s_waitcnt vmcnt(%0)" :: "n"(n) : "memory")
DI v4u selfrag(unsigned p, const v4u& mk) { return v4u{p & mk.x, p & mk.y, p & mk.z, p & mk.w}; }
DI v4u selmask(int c) { const int r7 = c & 7; const unsigned hm = (r7 & 1) ? 0xffff0000u : 0x0000ffffu;
  return v4u{(r7 >> 1) == 0 ? hm : 0u, (r7 >> 1) == 1 ? hm : 0u, (r7 >> 1) == 2 ? hm : 0u, (r7 >> 1) == 3 ? hm : 0u}; }
DI unsigned permlo(unsigned a, unsigned b) { return __builtin_amdgcn_perm(b, a, 0x05040100u); }
DI unsigned permhi(unsigned a, unsigned b) { return __builtin_amdgcn_perm(b, a, 0x07060302u); }
template <int N> DI unsigned badtags(const v4u (&u)[N], unsigned elo, unsigned ehi) {
  unsigned bad = 0;
#pragma unroll
  for (int k = 0; k < N; ++k) bad |= (u[k].y ^ elo) | (u[k].w ^ ehi);
  return bad;
}


constexpr int D = 640, HQ = 32, SK = 16384, DH = 128, WIN = 4096, DFF = 1728;
#define KV_WIN 8
#define KV_W1 6
#define KV_N1 4
#define AW_LO 5
#define AW_MID 6
#define AW_T44 660
#define AW_SL 128
#define AW_D 40
#define PV_AT 31
#define SV_WQ 6
#define SV_W 5
#define SV_WC 3
#define SV_WD 8
#define T_POLL1 370
#define T_POLLQ 720
#define T_POLLB 1000
#define T_D 125
#define AW_SET(n_) do { const int lag_ = (int)(__builtin_amdgcn_s_memrealtime() - tstart) - (AW_T44 + ((n_) - 44) * AW_SL / 10); lvl = lag_ < -AW_D / 2 ? 0 : (lag_ < AW_D / 2 ? 1 : 2); } while (0)
#define AW() do { if (lvl == 0) VMW(AW_LO); else if (lvl == 1) VMW(AW_MID); else VMW(KV_WIN); } while (0)
constexpr int WS_EPOCH = 0;
constexpr int WS_MP = 4096;
constexpr int WS_QF = WS_MP + 61440;
constexpr int WS_FL = WS_QF + 16384;
constexpr int WS_OP = WS_FL + 8192;
constexpr int WS_YP = WS_OP + 122880;
constexpr int WS_FP = WS_YP + 20480;
constexpr int WS_BYTES = WS_FP + 69120;
constexpr int L_K = 0;
constexpr int L_P = 147456;
constexpr int L_Q = L_P + 6656;
constexpr int L_OM = L_Q + 256;
constexpr int L_F = L_OM + 4096;
constexpr int L_END = L_F + 256;
enum { F_KA = 0  , F_KB = 4  , F_FREE = 8  , F_QRDY = 12, F_CML = 13, F_CP = 14, F_M = 16  , F_L = 20  , F_MCU = 24, F_LCU = 25 };
constexpr int S_W1 = 0;
constexpr int S_W2 = 81920;
constexpr int S_MB = 131072;
constexpr int S_S = S_MB + 4096;
constexpr int S_Y = S_S + 2560;
constexpr int S_Z = S_Y + 2048;
constexpr int S_X1 = S_Z + 2560;
constexpr int S_H = S_X1 + 1280;
constexpr int S_MV = S_H + 128;
constexpr int S_HT = S_MV + 7680;
constexpr int S_QM = 0;
constexpr int S_ST = S_HT + 2560;
static_assert(SV_WC <= SV_WD && SV_WD < 12, "");
enum { G_M1 = 0, G_M2 = 1, G_W1 = 2, G_W2 = 3, G_CV = 4, G_P1 = 5, G_P2 = 6, G_P3 = 7, G_Y = 8, G_H = 9, G_ARM = 10, G_Q = 11 };
static_assert(S_ST + 64 <= L_F && L_END <= 163840, "");

#define FOLD4(d_) (((d_).x + dpp<0x104>((d_).y)) + (dpp<0x108>((d_).z) + dpp<0x10C>((d_).w)))
DI float dot2(unsigned a, unsigned b, float acc) { return __builtin_amdgcn_fdot2_f32_bf16(__builtin_bit_cast(bf16x2, a), __builtin_bit_cast(bf16x2, b), acc, false); }
#define DMA(m0_, vo_, srd_, so_, pol_) do { unsigned keep_; \
    asm volatile("s_mov_b32 %0, m0\n\ts_mov_b32 m0, %1\n\ts_nop 0\n\tbuffer_load_dwordx4 %2, %3, %4 offen " pol_ " lds\n\ts_mov_b32 m0, %0" \
                 : "=&s"(keep_) : "s"(m0_), "v"(vo_), "s"(srd_), "s"(so_) : "memory"); } while (0)

DI v2u ld_epoch(rsrc_t rws, int lane) { return LD8(rws, lane == 0 ? 0 : OOB, WS_EPOCH, SC1); }
#define EPOCH_TAG(ep_, lo_, hi_) do { lo_ = (unsigned)__builtin_amdgcn_readfirstlane((ep_).x) + 1u; hi_ = (unsigned)__builtin_amdgcn_readfirstlane((ep_).y) + 1u; } while (0)
__global__ void __launch_bounds__(512, 2) fused_kernel(const u16* kc, const u16* vc, char* ws, const u16* wmod, const u16* cvec, const u16* x,
    const u16* wq, const u16* wo, const u16* w1, const u16* w2, u16* out) {
  __shared__ __attribute__((aligned(16))) char smem[L_END];
  LAS char* L = (LAS char*)smem;
  const int tid = threadIdx.x, lane = tid & 63, wave = __builtin_amdgcn_readfirstlane(tid >> 6);
  const int b = blockIdx.x, xg = b & 7, r = b >> 3;
  const int g = lane >> 4, c = lane & 15;
  const unsigned lds0 = (unsigned)(uintptr_t)L;
  const unsigned long long tstart = __builtin_amdgcn_s_memrealtime();
  unsigned long long pc0; unsigned pclen;
  asm volatile("s_getpc_b64 %0\n.Lpf_here:\n\ts_mov_b32 %1, .Lpf_end-.Lpf_here\n\ts_max_i32 %1, %1, .Lpf_end2-.Lpf_here" : "=s"(pc0), "=s"(pclen) :: "scc");

  if (__builtin_expect(r < 24, 1)) {
    volatile LAS unsigned* FL = (volatile LAS unsigned*)(L + L_F);
    const int hd = xg * 4 + (r & 3), sp = r >> 2, j = wave & 3;
    const int nr = sp < 4 ? 43 : 42, r0 = sp * 43 - (sp == 5 ? 1 : 0);
    const size_t kvoff = ((size_t)hd * SK + (SK - WIN) + r0 * 16) * DH;
    if (__builtin_expect(wave < 4, 1)) {
      unsigned pf[11];
      {
        const int t = 95 - (r * 4 + j);
        const rsrc_t rm = mkrs(wmod, D * 6 * D * 2), rq = mkrs(wq, D * HQ * DH * 2);
#pragma unroll
        for (int i = 0; i < 3; ++i) { const int n = t + 96 * i, um = xg * 5 + n / 40;
          pf[i] = LD4(rm, n < 200 ? (lane >> 3) * 7680 + (lane & 7) * 16 : OOB, ((um & 1) * 320 + 8 * (n % 40)) * 7680 + (um >> 1) * 128, 0); }
        pf[10] = LD4(mkrs((const void*)pc0, (int)pclen), (lane < 8 && t < 32) ? lane * 128 : OOB, t * 1024, 0);
#pragma unroll
        for (int i = 0; i < 7; ++i) pf[3 + i] = LD4(rq, lane * 16, (t + 96 * i) * 8192 + xg * 1024, 0);
        SB(); CBAR();
      }
      const unsigned long long ka = (unsigned long long)(kc + kvoff);
      const v4u srdK = {(unsigned)ka, (unsigned)(ka >> 32), (unsigned)nr * 4096u, 0x00020000u};
      int vo[4];
#pragma unroll
      for (int q = 0; q < 4; ++q) vo[q] = g * 256 + ((c ^ (4 * q + g)) * 16);
      const unsigned lbase = lds0 + L_K + j * 36864;
      int lvl = 1;
      asm volatile("s_nop 4" ::: "memory");
#pragma nounroll
      for (int i4 = 0; i4 < 11; ++i4) {
        if (i4 == 9) lds_wait(FL + F_FREE + j, 1);
        const unsigned mb = lbase + (i4 < 9 ? i4 : i4 - 9) * 4096, sb = i4 * 16384 + j * 1024;
#pragma unroll
        for (int q = 0; q < 4; ++q) { DMA(mb + q * 1024, vo[q], srdK, sb + q * 4096, "nt"); if (q == 0 && i4 == 0) __builtin_amdgcn_s_barrier();   if (i4 < KV_N1) VMW(KV_W1); else AW(); }
        AW_SET(i4 * 4 + 4);
        if (i4 == (8 + KV_WIN + 3) / 4 - 1) FL[F_KA + j] = 1u;
      }
      const rsrc_t rws = mkrs(ws, WS_BYTES);
      const rsrc_t rv = mkrs(vc + kvoff, nr * 4096);
      const v2u ep = ld_epoch(rws, lane);
      v4u V[43];
#pragma unroll
      for (int i = 0; i <= PV_AT; ++i) {
        V[i] = LD16(rv, lane * 16, i * 4096 + j * 1024, NT);
        AW();
        if ((i & 3) == 3) AW_SET(45 + i);
        if (i == KV_WIN - 1) { FL[F_KB + j] = 1u; }
        SB();
      }
#pragma unroll
      for (int i = 0; i < 11; ++i) asm volatile("" :: "v"(pf[i]));
      unsigned elo, ehi;
      EPOCH_TAG(ep, elo, ehi);
      const v4u mk = selmask(c);
      int sto = lane < 32 ? c * 32 + g * 16 : (lane == 32 ? 512 : OOB);
      PIN(sto);
      lds_wait(FL + F_CP, 4);
      const LAS v4u* pt = (const LAS v4u*)(L + L_P + (((j * 2 + (c >> 3)) * 4 + g) * 52) * 4);
      v4u p4[11];
#pragma unroll
      for (int i4 = 0; i4 < 11; ++i4) p4[i4] = pt[i4];
      unsigned mcu = FL[F_MCU], lcu = FL[F_LCU];
      SB();
      f32x4 acc = {0.f, 0.f, 0.f, 0.f};
      constexpr int PER = (43 - KV_WIN - 1) / (42 - PV_AT);
#pragma unroll
      for (int i = PV_AT + 1; i < 43; ++i) {
        V[i] = LD16(rv, lane * 16, i * 4096 + j * 1024, NT);
        SB();
#pragma unroll
        for (int u = 0; u < PER; ++u) { const int n = (i - PV_AT - 1) * PER + u; acc = mfma(selfrag(p4[n >> 2][n & 3], mk), V[n], acc); }
        SB();
        AW();
        SB();
      }
      PIN(mcu); PIN(lcu);
#pragma unroll
      for (int n = (42 - PV_AT) * PER; n < 43; ++n) acc = mfma(selfrag(p4[n >> 2][n & 3], mk), V[n], acc);
      ((LAS f32x4*)(L + L_OM))[j * 64 + lane] = acc;
      BARRIER();
      if (j == 0) {
        const LAS f32x4* om = (const LAS f32x4*)(L + L_OM) + (lane & 31);
        const f32x4 o = ((om[0] + om[32]) + (om[64] + om[96])) + ((om[128] + om[160]) + (om[192] + om[224]));
        const bool ml = lane >= 32;
        const v4u st = {ml ? mcu : cvtpk(o.x, o.y), elo, ml ? lcu : cvtpk(o.z, o.w), ehi};
        ST16(st, rws, sto, WS_OP + (hd * 6 + sp) * 640, SC1);
      }
      return;
    }
    const rsrc_t rws = mkrs(ws, WS_BYTES);
    v2u ep = ld_epoch(rws, lane);
    FL[lane] = 0u;
    BARRIER();
    const LAS char* kb = L + L_K + j * 36864 + c * 256;
    v4u kf[11][4];
    lds_wait(FL + F_KA + j, 1);
#pragma unroll
    for (int t = 0; t < 2; ++t)
#pragma unroll
      for (int ks = 0; ks < 4; ++ks) kf[t][ks] = *(const LAS v4u*)(kb + t * 4096 + (((4 * ks + g) ^ c) * 16));
    SB();
    LGKM0();
    FL[F_FREE + j] = 1u;
    if (j == 0) {
      unsigned elo, ehi;
      EPOCH_TAG(ep, elo, ehi);
#pragma nounroll
      for (int it = 0; it < SPIN * 4; ++it) { if (__builtin_amdgcn_s_memrealtime() - tstart >= T_POLLQ) break; __builtin_amdgcn_s_sleep(2); }
      v4u qu[1];
      bool bd = lane < 32;
#pragma nounroll
      for (int it = 0; it < SPIN; ++it) {
        CBAR();
        if (bd) { qu[0] = LD16(rws, lane * 16, WS_QF + hd * 512, SC1); bd = badtags<1>(qu, elo, ehi) != 0u; }
        if (none(bd)) break;
      }
      if (lane < 32) ((LAS v2u*)(L + L_Q))[lane] = v2u{qu[0].x, qu[0].z};
      LGKM0();
      FL[F_QRDY] = 1u;
    }
    lds_wait(FL + F_KB + j, 1);
#pragma unroll
    for (int t = 2; t < 11; ++t)
#pragma unroll
      for (int ks = 0; ks < 4; ++ks) kf[t][ks] = *(const LAS v4u*)(kb + (t < 9 ? t : t - 9) * 4096 + (((4 * ks + g) ^ c) * 16));
    SB();
    lds_wait(FL + F_QRDY, 1);
    v4u qa[4];
#pragma unroll
    for (int ks = 0; ks < 4; ++ks) qa[ks] = *(const LAS v4u*)(L + L_Q + (4 * ks + g) * 16);
    float sc[11];
#pragma unroll
    for (int t = 0; t < 11; ++t) {
      f32x4 d = {0.f, 0.f, 0.f, 0.f};
#pragma unroll
      for (int ks = 0; ks < 4; ++ks) d = mfma(qa[ks], kf[t][ks], d);
      sc[t] = d.x;
    }
#pragma unroll
    for (int t = 0; t < 11; ++t) PIN(sc[t]);
    const bool g1 = g & 1, g2 = g & 2;
    float s0 = g2 ? (g1 ? sc[3] : sc[2]) : (g1 ? sc[1] : sc[0]);
    float s1 = g2 ? (g1 ? sc[7] : sc[6]) : (g1 ? sc[5] : sc[4]);
    float s2 = g2 ? sc[10] : (g1 ? sc[9] : sc[8]);
    s0 = rbf(rbf(s0) * 0.08838834764831845f); s1 = rbf(rbf(s1) * 0.08838834764831845f); s2 = rbf(rbf(s2) * 0.08838834764831845f);
    const bool v2 = 128 + lane < 4 * nr;
    if (!v2) s2 = -INFINITY;
    const float m = wmax(fmaxf(fmaxf(s0, s1), s2));
    float p0 = __expf(s0 - m), p1 = __expf(s1 - m), p2 = v2 ? __expf(s2 - m) : 0.f;
    const float l = wsum((p0 + p1) + p2);
    if (lane == 0) { FL[F_M + j] = F2U(m); FL[F_L + j] = F2U(l); }
    lds_arrive((LAS unsigned*)(L + L_F) + F_CML, lane);
    lds_wait(FL + F_CML, 4);
    {
      const f32x4 m4 = *(const LAS f32x4*)(L + L_F + F_M * 4), l4 = *(const LAS f32x4*)(L + L_F + F_L * 4);
      const float mcu = fmaxf(fmaxf(m4.x, m4.y), fmaxf(m4.z, m4.w));
      const float lcu = (l4.x * __expf(m4.x - mcu) + l4.y * __expf(m4.y - mcu)) + (l4.z * __expf(m4.z - mcu) + l4.w * __expf(m4.w - mcu));
      const float f = __expf(m - mcu) / lcu;
      p0 *= f; p1 *= f; p2 *= f;
      if (lane == 0 && j == 0) { FL[F_MCU] = F2U(mcu); FL[F_LCU] = F2U(lcu); }
    }
    const unsigned h0 = cvtpk(p0, p0), h1 = cvtpk(p1, p1), h2 = cvtpk(p2, p2);
    const float e0 = p0 - bflo(h0), e1 = p1 - bflo(h1), e2 = p2 - bflo(h2);
    LAS unsigned* pw = (LAS unsigned*)(L + L_P) + (j * 8 + (lane & 3)) * 52 + (lane >> 2);
    pw[0] = h0; pw[16] = h1; pw[32] = h2; pw[208] = cvtpk(e0, e0); pw[224] = cvtpk(e1, e1); pw[240] = cvtpk(e2, e2);
    lds_arrive((LAS unsigned*)(L + L_F) + F_CP, lane);
    return;
  }

  const int a = wave & 3, k = r - 24;
  const bool role_b = k < 5, role_c = k >= 5 || (k == 0 && xg < 3), role_m = !(k == 0 && xg < 4);
  const int uc = k >= 5 ? xg * 3 + k - 5 : 24 + xg;
  const int chi = c & 7, rho = c >> 3;
  const rsrc_t rws = mkrs(ws, WS_BYTES);
  const rsrc_t rx = mkrs(x, D * 2);
  volatile LAS unsigned* FL = (volatile LAS unsigned*)(L + L_F);
  LAS unsigned* FC = (LAS unsigned*)(L + L_F);
  auto red4 = [&](f32x4 t) { return sw32(sw16(t.x, t.y), sw16(t.z, t.w)); };
  unsigned elo, ehi;
  const bool role_q = k >= 1 && k <= 4;

  if (wave < 4) {
    const int hb = xg * 4 + a;
    const v2u ep = ld_epoch(rws, lane);
    __builtin_amdgcn_s_barrier();
    EPOCH_TAG(ep, elo, ehi);
    if (role_q) {
      v4u WQ[40];
      const rsrc_t rq = mkrs(wq, D * HQ * DH * 2);
#pragma unroll
      for (int i = 0; i < 40; ++i) { WQ[i] = LD16(rq, (40 * g) * 8192 + c * 16, (160 * a + i) * 8192 + (xg * 4 + k - 1) * 256, 0); VMW(SV_WQ); SB(); }
      const v4u mk = selmask(c);
      lds_wait(FL + G_H, 4);
      const LAS v4u* ht = (const LAS v4u*)(L + S_HT + (160 * a + 40 * g) * 4);
      f32x4 acc = {0.f, 0.f, 0.f, 0.f}, acc2 = {0.f, 0.f, 0.f, 0.f};
      v4u h4[10];
#pragma unroll
      for (int i = 0; i < 10; ++i) h4[i] = ht[i];
      SB();
#pragma unroll
      for (int i = 0; i < 40; i += 2) { acc = mfma(selfrag(h4[i >> 2][i & 3], mk), WQ[i], acc); acc2 = mfma(selfrag(h4[i >> 2][(i & 3) + 1], mk), WQ[i + 1], acc2); }
      if (lane < 32) ((LAS f32x4*)(L + S_QM))[a * 32 + c * 2 + g] = acc + acc2;
      lds_arrive(FC + G_Q, lane);
      lds_wait(FL + G_Q, 4);
      if (a == 0) {
        const LAS f32x2* mb = (const LAS f32x2*)(L + S_QM) + lane;
        const f32x2 qv = (mb[0] + mb[64]) + (mb[128] + mb[192]);
        const unsigned qp = cvtpk(qv.x, qv.y), qn = F2U(dpp<XOR1>(U2F(qp)));
        ST16((v4u{qp, elo, qn, ehi}), rws, !(lane & 1) ? lane * 8 : OOB, WS_QF + (xg * 4 + k - 1) * 512, SC1);
      } else __builtin_amdgcn_s_sleep(6);
    } else if (k == 5) lds_wait(FL + G_M1, 5);
    v4u WB[32];
    if (role_b) {
      const rsrc_t ro = mkrs(wo, HQ * DH * D * 2);
#pragma unroll
      for (int i = 0; i < 32; ++i) {
        WB[i] = LD16(ro, (4 * g) * 1280 + c * 16, (hb * 128 + 64 * (i >> 4) + 32 * ((i >> 2) & 1) + 16 * ((i >> 3) & 1) + (i & 3)) * 1280 + k * 256, NT);
        if (role_c) VMW(SV_WD); else VMW(SV_W);
        SB();
      }
    }
    if (role_c) {
      const unsigned long long wa = (unsigned long long)w1, wb = (unsigned long long)(w2 + (size_t)uc * 64 * D);
      const v4u srd1 = {(unsigned)wa, (unsigned)(wa >> 32), (unsigned)(D * DFF * 2), 0x00020000u};
      const v4u srd2 = {(unsigned)wb, (unsigned)(wb >> 32), 81920u, 0x00020000u};
      const int f0 = (g & 1) | ((a & 1) << 2);
      const int voa = (lane >> 3) * 3456 + (((lane & 7) ^ f0) * 16), vob = (lane >> 3) * 3456 + (((lane & 7) ^ f0 ^ 2) * 16);
      asm volatile("s_nop 4" ::: "memory");
#pragma nounroll
      for (int i = 0; i < 20; i += 2) {
        const unsigned m0v = lds0 + S_W1 + (a * 20 + i) * 1024, so = (a * 160 + i * 8) * 3456 + uc * 128;
        DMA(m0v, voa, srd1, so, "nt"); if (role_b) VMW(SV_WD); else VMW(SV_WC); DMA(m0v + 1024, vob, srd1, so + 8 * 3456, "nt"); if (role_b) VMW(SV_WD); else VMW(SV_WC);
      }
#pragma nounroll
      for (int i = 0; i < 20; ++i) {
        const int p = 4 * i + a, row = ((4 * p + g) * 205) >> 10;
        const int vo2 = g * 256 + ((c ^ (((row & 3) | ((row >> 1) & 4)) << 1)) * 16);
        if (i == 12) lds_wait(FL + G_CV, 4);
        const unsigned m0v = lds0 + (i < 12 ? S_W2 + p * 1024 : S_W1 + (p - 48) * 1024), so = p * 1024;
        DMA(m0v, vo2, srd2, so, "nt"); if (role_b) VMW(SV_WD); else VMW(SV_WC);
        if (i == SV_WD) lds_arrive(FC + G_W1, lane);
      }
      VMW(0);
      lds_arrive(FC + G_W2, lane);
    }
    if (!role_b) return;
    v4u BF[4][8];
#pragma unroll
    for (int hf = 0; hf < 4; ++hf) {
#pragma unroll
      for (int m = 0; m < 4; ++m)
#pragma unroll
        for (int d = 0; d < 4; ++d) { BF[hf][2 * d][m] = permlo(WB[hf * 8 + 2 * m][d], WB[hf * 8 + 2 * m + 1][d]); BF[hf][2 * d + 1][m] = permhi(WB[hf * 8 + 2 * m][d], WB[hf * 8 + 2 * m + 1][d]); }
#pragma unroll
      for (int t = 0; t < 8; ++t) PIN(BF[hf][t]);
    }
    const bool role_d = (a == 1 && k > 0) || (a == 2 && k == 1);
    const int v = xg * 5 + (a == 1 ? k : 0);
    const int q4 = lane & 3, part = lane >> 2;
    float dx = 0.f, dga = 0.f, dgb = 0.f;
    if (role_d) {
      const unsigned xv = LD4(rx, q4 * 8 + (g >> 1) * 4, v * 32, 0);
      v4u mm[2];
      bool bd = true;
#pragma nounroll
      for (int it = 0; it < SPIN; ++it) {
        CBAR();
        if (bd) {
          mm[0] = LD16(rws, ((lane >> 3) & 1) * 30720 + ((lane >> 4) & 1) * 15360 + (lane & 7) * 16, WS_MP + 10240 + v * 128, SC1);
          mm[1] = LD16(rws, lane * 128, WS_FL, SC1);
          bd = badtags<2>(mm, elo, ehi) != 0u;
        }
        if (none(bd)) break;
        __builtin_amdgcn_s_sleep(16);
      }
      const bool odd = g & 1;
      dx = odd ? bfhi(xv) : bflo(xv);
      const float e0 = U2F(mm[0].x) + dpp<ROR8>(U2F(mm[0].x)), e1 = U2F(mm[0].z) + dpp<ROR8>(U2F(mm[0].z));
      const int src = q4 * 2 + (g >> 1);
      const float a0 = __shfl(e0, src), a1 = __shfl(e1, src), b0 = __shfl(e0, 16 + src), b1 = __shfl(e1, 16 + src);
      dga = rbf(odd ? a1 : a0); dgb = rbf(odd ? b1 : b0);
      PIN(dx); PIN(dga); PIN(dgb);
    }
    lds_wait(FL + G_ARM, 1);
#pragma nounroll
    for (int it = 0; it < SPIN * 4; ++it) { if (__builtin_amdgcn_s_memrealtime() - tstart >= T_POLLB) break; __builtin_amdgcn_s_sleep(4); }
    {
      const int s2 = (c + 4 * g) & 7;
      int vo1 = (c & 7) * 640 + (c >> 3) * 64 + g * 16, vo2 = s2 * 640 + 512;
      unsigned m_lo = c < 6 ? ~0u : 0u, m_hi = (c >= 8 && c < 14) ? ~0u : 0u;
      int lw = S_Y + (a * 128 + c * 8 + g) * 4;
      PIN(vo1); PIN(vo2); PIN(lw); PIN(m_lo); PIN(m_hi);
      v4u oa[4], ml[1];
      ml[0] = v4u{0xff800000u, elo, 0u, ehi};
#pragma unroll
      for (int u = 0; u < 4; ++u) oa[u] = v4u{0u, elo, 0u, ehi};
      bool b1 = (c & 7) < 6, b2 = s2 < 6;
#pragma nounroll
      for (int it = 0; it < SPIN; ++it) {
        CBAR();
        if (b2) ml[0] = LD16(rws, vo2, WS_OP + hb * 3840, SC1);
        if (b1) {
#pragma unroll
          for (int u = 0; u < 4; ++u) oa[u] = LD16(rws, vo1, WS_OP + hb * 3840 + u * 128, SC1);
        }
        b2 = badtags<1>(ml, elo, ehi) != 0u; b1 = badtags<4>(oa, elo, ehi) != 0u;
        if (none(b1 | b2)) break;
      }
      f32x4 d[8];
#pragma unroll
      for (int t = 0; t < 8; ++t) d[t] = f32x4{0.f, 0.f, 0.f, 0.f};
#pragma unroll
      for (int hf = 0; hf < 4; ++hf) {
        const unsigned m_ = (hf & 1) ? m_hi : m_lo;
        const v4u af = {oa[hf & 2].x & m_, oa[hf & 2].z & m_, oa[(hf & 2) + 1].x & m_, oa[(hf & 2) + 1].z & m_};
#pragma unroll
        for (int t = 0; t < 8; ++t) d[t] = mfma(af, BF[hf][t], d[t]);
      }
      const float ms = U2F(ml[0].x);
      float M = fmaxf(ms, dpp<XOR1>(ms)); M = fmaxf(M, dpp<XOR2>(M)); M = fmaxf(M, dpp<XOR7>(M));
      const float e = U2F(ml[0].z) * __expf(ms - M);
      float Ls = e + dpp<XOR1>(e); Ls += dpp<XOR2>(Ls); Ls += dpp<XOR7>(Ls);
      const float wt = e * __builtin_amdgcn_rcpf(Ls);
      const float w0 = dpp<BCAST + 0>(wt), w1_ = dpp<BCAST + 1>(wt), w2_ = dpp<BCAST + 2>(wt), w3 = dpp<BCAST + 3>(wt);
      float y[8];
#pragma unroll
      for (int t = 0; t < 8; ++t) y[t] = fmaf(d[t].w, w3, fmaf(d[t].z, w2_, fmaf(d[t].y, w1_, d[t].x * w0)));
      const float ya = sw32(sw16(y[0], y[1]), sw16(y[2], y[3])), yb = sw32(sw16(y[4], y[5]), sw16(y[6], y[7]));
      *(LAS float*)(L + lw) = ya; *(LAS float*)(L + lw + 16) = yb;
      CBAR();
      if (lane == 0) __hip_atomic_fetch_add(FC + G_Y, 1u, __ATOMIC_RELAXED, __HIP_MEMORY_SCOPE_WORKGROUP);
      CBAR();
      if (a == 0) {
        const volatile LAS f32x2* tp = (const volatile LAS f32x2*)(L + S_Y) + lane;
        f32x2 t0, t1, t2, t3;
#pragma nounroll
        for (int it = 0; it < SPIN * 8; ++it) {
          const unsigned cnt = FL[G_Y];
          t0 = tp[0]; t1 = tp[64]; t2 = tp[128]; t3 = tp[192];
          if (__builtin_amdgcn_readfirstlane(cnt) >= 4u) break;
        }
        const f32x2 yy = (t0 + t1) + (t2 + t3);
        ST8((v2u{nosentf(yy.x), nosentf(yy.y)}), rws, lane * 8, WS_YP + xg * 2560 + k * 512, SC1);
      }
    }
    if (role_d) {
      v4u yq;
#pragma nounroll
      for (int it = 0; it < SPIN; ++it) {
        CBAR();
        yq = LD16(rws, q4 * 16 + (part & 7) * 2560, WS_YP + v * 64, SC1);
        if (none(mx4(yq) == SENT)) break;
        __builtin_amdgcn_s_sleep(2);
      }
      const unsigned long long ty = __builtin_amdgcn_s_memrealtime();
      auto red = [&](f32x4 t) { float s = red4(t); s += dpp<ROR4>(s); s += dpp<ROR8>(s); return s; };
      float x1 = rbf(dx + rbf(dga * rbf(0.5f * red(F4(yq)))));
      int o0 = q4 * 16 + part * 2560;
      PIN(x1); PIN(o0);
      v4u f0, f1 = {0u, 0u, 0u, 0u};
      bool b0 = true, b1 = part < 11;
#pragma nounroll
      for (int it = 0; it < SPIN; ++it) { if (__builtin_amdgcn_s_memrealtime() - ty >= T_D) break; }
#pragma nounroll
      for (int it = 0; it < SPIN; ++it) {
        CBAR();
        if (b0) f0 = LD16(rws, o0, WS_FP + v * 64, SC1);
        if (b1) f1 = LD16(rws, o0, WS_FP + v * 64 + 16 * 2560, SC1);
        b0 = mx4(f0) == SENT; b1 = mx4(f1) == SENT;
        if (none(b0 | b1)) break;
      }
      const float mo = red(F4(f0) + F4(f1));
      const float res = x1 + rbf(dgb * rbf(mo));
      if (c < 4) ((__bf16*)out)[v * 16 + q4 * 4 + g] = (__bf16)res;
      if (v == 0 && lane == 0) ST8((v2u{elo, ehi}), rws, 0, WS_EPOCH, SC1);
      asm volatile(".Lpf_end2:");
    }
    return;
  }

  {
    const int u1 = (k >= 1 && k <= 5) ? xg * 5 + k - 1 : (k == 0 ? 72 + xg * 2 : 48 + xg * 4 + (k - 6) * 2);
    const int u2 = role_q ? 88 + xg * 4 + k - 1 : (k == 5 ? 40 + xg : u1 + 1);
    const rsrc_t rm = mkrs(wmod, D * 6 * D * 2);
    const int mvo = (10 * (2 * g + rho)) * 7680 + chi * 16;
    v4u WM[10];
    const v4u mk = selmask(c);
    auto mod_issue = [&](int um) {
#pragma unroll
      for (int i = 0; i < 10; ++i) WM[i] = LD16(rm, mvo, (320 * (um & 1) + 80 * a + i) * 7680 + (um >> 1) * 128, 0);
      SB(); CBAR();
    };
    auto mod_unit = [&](int u, int cnt, int um) {
      const LAS v2u* sp_ = (const LAS v2u*)(L + S_S) + (um & 1) * 160 + a * 40 + 5 * (2 * g + rho);
      v2u sv2[5];
#pragma unroll
      for (int i = 0; i < 5; ++i) sv2[i] = sp_[i];
      f32x4 acc = {0.f, 0.f, 0.f, 0.f};
#pragma unroll
      for (int i = 0; i < 5; ++i) { acc = mfma(selfrag(sv2[i].x, mk), WM[2 * i], acc); acc = mfma(selfrag(sv2[i].y, mk), WM[2 * i + 1], acc); }
      if ((g >> 1) == rho) *(LAS f32x4*)(L + S_MB + u * 2048 + ((a * 2 + rho) * 16 + chi * 2 + (g & 1)) * 16) = acc;
      lds_arrive(FC + cnt, lane);
      if (a == 0) {
        lds_wait(FL + cnt, 4);
        const LAS f32x2* mb = (const LAS f32x2*)(L + S_MB + u * 2048) + (lane & 31);
        const f32x2 mv = ((mb[0] + mb[32]) + (mb[64] + mb[96])) + ((mb[128] + mb[160]) + (mb[192] + mb[224]));
        ST16((v4u{F2U(mv.x), elo, F2U(mv.y), ehi}), rws, lane < 32 ? lane * 16 : OOB, WS_MP + (um & 1) * 30720 + (um >> 1) * 512, SC1);
        lds_arrive(FC + cnt, lane);
      }
    };
    FL[lane] = 0u;
    v2u ep = ld_epoch(rws, lane);
    unsigned xr[5];
#pragma unroll
    for (int i = 0; i < 5; ++i) xr[i] = LD4(rx, lane * 4, i * 256, 0);
    const unsigned xa = LD4(rx, lane * 4, a * 256, 0), xb = LD4(rx, c * 4, 1024 + a * 64, 0);
    if (role_m) {
      const v2u cv = LD8(mkrs(cvec, D * 2), lane < 40 ? (lane >= 20 ? 640 + (lane - 20) * 8 : lane * 8) : OOB, a * 160, 0);
      mod_issue(u1);
      BARRIER();
      float cf[4] = {bflo(cv.x), bfhi(cv.x), bflo(cv.y), bfhi(cv.y)};
      unsigned sd[4];
#pragma unroll
      for (int i = 0; i < 4; ++i) { const float sv = cf[i] / (1.f + __expf(-cf[i])); sd[i] = cvtpk(sv, sv); }
      if (lane < 40) ((LAS v4u*)(L + S_S))[(lane >= 20 ? 60 + lane : lane) + a * 20] = v4u{sd[0], sd[1], sd[2], sd[3]};
      LGKM0();
      PIN(ep);
      EPOCH_TAG(ep, elo, ehi);
      mod_unit(0, G_M1, u1);
    } else {
      BARRIER();
      PIN(ep);
      EPOCH_TAG(ep, elo, ehi);
    }
    if (a == 3) {
      const v4u sv = {SENT, SENT, SENT, SENT};
      ST16(sv, rws, (role_b && lane < 32) ? lane * 16 : OOB, WS_YP + xg * 2560 + k * 512, SC1);
      if (role_c) { ST16(sv, rws, lane * 16, WS_FP + uc * 2560, SC1); ST16(sv, rws, lane * 16, WS_FP + uc * 2560 + 1024, SC1); ST16(sv, rws, lane < 32 ? lane * 16 : OOB, WS_FP + uc * 2560 + 2048, SC1); }
    }
    if (role_q) {
      float s = 0.f;
#pragma unroll
      for (int i = 0; i < 5; ++i) s += bflo(xr[i]) + bfhi(xr[i]);
      float mean = wsum(s) * (1.f / D);
      float v = 0.f;
#pragma unroll
      for (int i = 0; i < 5; ++i) { const float d0 = bflo(xr[i]) - mean, d1 = bfhi(xr[i]) - mean; v += d0 * d0 + d1 * d1; }
      v = wsum(v) * (1.f / D);
      const float den = rbf(sqrtf(rbf(rbf(v) + 1e-5f)));
      mean = rbf(mean);
      float n0 = rbf(rbf(bflo(xa) - mean) / den), n1 = rbf(rbf(bfhi(xa) - mean) / den), n2 = rbf(rbf(bflo(xb) - mean) / den), n3 = rbf(rbf(bfhi(xb) - mean) / den);
      PIN(n0); PIN(n1); PIN(n2); PIN(n3);
      v4u u[5];
      bool bd = true;
#pragma nounroll
      for (int it = 0; it < SPIN * 4; ++it) { if (__builtin_amdgcn_s_memrealtime() - tstart >= T_POLL1) break; __builtin_amdgcn_s_sleep(1); }
#pragma nounroll
      for (int it = 0; it < SPIN; ++it) {
        CBAR();
        if (bd) {
#pragma unroll
          for (int i = 0; i < 4; ++i) u[i] = LD16(rws, lane * 16, WS_MP + (i & 1) * 30720 + (i >> 1) * 5120 + a * 1024, SC1);
          u[4] = LD16(rws, (g & 1) * 30720 + (g >> 1) * 5120 + c * 16, WS_MP + 4096 + a * 256, SC1);
          bd = badtags<5>(u, elo, ehi) != 0u;
        }
        if (none(bd)) break;
      }
      const float sa0 = rbf(U2F(u[0].x) + U2F(u[1].x)), sa1 = rbf(U2F(u[0].z) + U2F(u[1].z)), ca0 = rbf(U2F(u[2].x) + U2F(u[3].x)), ca1 = rbf(U2F(u[2].z) + U2F(u[3].z));
      const float h0 = rbf(n0 * rbf(1.f + ca0)) + sa0, h1 = rbf(n1 * rbf(1.f + ca1)) + sa1;
      ((LAS v2u*)(L + S_HT))[a * 64 + lane] = v2u{cvtpk(h0, h0), cvtpk(h1, h1)};
      const float e0 = rbf(x16sum(U2F(u[4].x))), e1 = rbf(x16sum(U2F(u[4].z)));
      auto s0 = __builtin_amdgcn_permlane32_swap(F2U(e0), F2U(e0), false, false); auto s1 = __builtin_amdgcn_permlane32_swap(F2U(e1), F2U(e1), false, false);
      const float h2 = rbf(n2 * rbf(1.f + U2F(s0[1]))) + U2F(s0[0]), h3 = rbf(n3 * rbf(1.f + U2F(s1[1]))) + U2F(s1[0]);
      if (lane < 16) ((LAS v2u*)(L + S_HT))[256 + a * 16 + lane] = v2u{cvtpk(h2, h2), cvtpk(h3, h3)};
      lds_arrive(FC + G_H, lane);
      lds_wait(FL + G_H, 4);
    }
    if (role_m) { mod_issue(u2); mod_unit(1, G_M2, u2); }
    if (a == 3) {
      VMW(0);
      ST16((v4u{0u, elo, 0u, ehi}), rws, lane == 0 ? 0 : OOB, WS_FL + (xg * 8 + k) * 128, SC1);
      FL[G_ARM] = 1u;
    }
  }
  if (!role_c) return;
  typedef short v4s __attribute__((ext_vector_type(4)));
#define TRRD(p_) __builtin_bit_cast(v2u, __builtin_amdgcn_ds_read_tr16_b64_v4i16((LAS v4s*)(p_)))
  v4u B1[5][4];
  v4u B2[2][10];
  const int q_ = c >> 2, p_ = c & 3;
  lds_wait(FL + G_W1, 4);
  {
    const int f_ = (q_ >> 1) | ((g & 1) << 1) | ((p_ & 1) << 2);
    LAS char* wi = L + S_W1 + (160 * p_ + 8 * g + q_) * 128;
#pragma unroll
    for (int t = 0; t < 4; ++t) {
      LAS char* wt = wi + (((2 * a + (t >> 1)) ^ f_) * 16) + (t & 1) * 8;
#pragma unroll
      for (int ks = 0; ks < 5; ++ks) { const v2u lo = TRRD(wt + ks * 4096), hi = TRRD(wt + ks * 4096 + 512); B1[ks][t] = v4u{lo.x, lo.y, hi.x, hi.y}; }
    }
#pragma unroll
    for (int ks = 0; ks < 5; ++ks)
#pragma unroll
      for (int t = 0; t < 4; ++t) PIN(B1[ks][t]);
  }
  lds_arrive(FC + G_CV, lane);
  const int el = 32 * a + 4 * chi + g;
  float fa[5], fb[5], Cc, Ss;
  const LAS char* zr = L + S_Z + p_ * 320 + g * 16;
  {
    unsigned xe[5];
#pragma unroll
    for (int i = 0; i < 5; ++i) xe[i] = LD4(rx, (el >> 1) * 4, i * 256, 0);
    v4u mm[9];
    const int c3 = a < 3 ? a + 12 : a;
    bool bd = true, bt = lane < 16;
    mm[8] = v4u{0u, elo, 0u, ehi};
#pragma nounroll
    for (int it = 0; it < SPIN; ++it) {
      CBAR();
      if (bd) {
#pragma unroll
        for (int i = 0; i < 8; ++i) mm[i] = LD16(rws, lane * 16, WS_MP + 10240 + (i & 1) * 30720 + (i < 6 ? a + 4 * (i >> 1) : c3) * 1024, SC1);
      }
      if (bt) mm[8] = LD16(rws, lane * 128, WS_FL + a * 2048, SC1);
      bd = badtags<9>(mm, elo, ehi) != 0u; bt = bt && bd;
      if (none(bd)) break;
      __builtin_amdgcn_s_sleep(16);
    }
#pragma unroll
    for (int i = 0; i < 4; ++i) *(LAS f32x2*)(L + S_MV + (i < 3 ? a + 4 * i : c3) * 512 + lane * 8) = f32x2{rbf(U2F(mm[2 * i].x) + U2F(mm[2 * i + 1].x)), rbf(U2F(mm[2 * i].z) + U2F(mm[2 * i + 1].z))};
    lds_arrive(FC + G_P1, lane);
    lds_wait(FL + G_P1, 4);
#pragma unroll
    for (int i = 0; i < 5; ++i) {
      const LAS float* mv = (const LAS float*)(L + S_MV) + el + 128 * i;
      const float gav = mv[0], sbv = mv[640], c1 = rbf(1.f + mv[1280]);
      const float xv = (g & 1) ? bfhi(xe[i]) : bflo(xe[i]);
      fa[i] = rho ? gav : gav * c1; fb[i] = rho ? xv : xv * c1;
      *(LAS __bf16*)(L + S_Z + rho * 1280 + (el + 128 * i) * 2) = (__bf16)(rho ? sbv : c1);
      PIN(fa[i]); PIN(fb[i]);
    }
    lds_arrive(FC + G_P2, lane);
    lds_wait(FL + G_P2, 4);
    f32x4 dc[4], ds[4];
#pragma unroll
    for (int t = 0; t < 4; ++t) { dc[t] = f32x4{0.f, 0.f, 0.f, 0.f}; ds[t] = dc[t]; }
#pragma unroll
    for (int ks = 0; ks < 5; ++ks) {
      const v4u ac = *(const LAS v4u*)(zr + ks * 64), as = *(const LAS v4u*)(zr + 1280 + ks * 64);
#pragma unroll
      for (int t = 0; t < 4; ++t) { dc[t] = mfma(ac, B1[ks][t], dc[t]); ds[t] = mfma(as, B1[ks][t], ds[t]); }
    }
    const bool g1 = g & 1, g2 = g & 2;
#define SEL4(d_) f32x4{g2 ? (g1 ? d_[3].x : d_[2].x) : (g1 ? d_[1].x : d_[0].x), g2 ? (g1 ? d_[3].y : d_[2].y) : (g1 ? d_[1].y : d_[0].y), \
                       g2 ? (g1 ? d_[3].z : d_[2].z) : (g1 ? d_[1].z : d_[0].z), g2 ? (g1 ? d_[3].w : d_[2].w) : (g1 ? d_[1].w : d_[0].w)}
#pragma unroll
    for (int t = 0; t < 4; ++t) { PIN(dc[t]); PIN(ds[t]); }
    { const f32x4 u = SEL4(dc), v = SEL4(ds); Cc = FOLD4(u); Ss = FOLD4(v); }
    PIN(Cc); PIN(Ss);
    lds_arrive(FC + G_P3, lane);
  }
  lds_wait(FL + G_W2, 4);
  {
    const int f2 = (q_ | ((g & 1) << 2)) << 1;
#pragma unroll
    for (int t = 0; t < 10; ++t) {
      const int cb = 320 * a + 32 * t + 8 * p_;
      const int cs = (cb & ~255) | ((((cb >> 4) & 15) ^ f2) << 4) | (cb & 8);
#pragma unroll
      for (int ks = 0; ks < 2; ++ks) {
        v2u h[2];
#pragma unroll
        for (int rd = 0; rd < 2; ++rd) { const int o = (32 * ks + 8 * g + 4 * rd + q_) * 1280 + cs; h[rd] = TRRD(L + (o < 49152 ? S_W2 + o : S_W1 + o - 49152)); }
        B2[ks][t] = v4u{h[0].x, h[0].y, h[1].x, h[1].y};
      }
      PIN(B2[0][t]); PIN(B2[1][t]);
    }
  }
  lds_wait(FL + G_P3, 4);
  {
    int zw = (rho ? S_X1 : S_Z) + el * 2;
    int hw = c < 4 ? S_H + (a * 16 + 4 * g + c) * 2 : S_ST + 16;
    int yo = (lane >> 3) * 2560 + a * 128 + (lane & 7) * 16;
    int st3 = lane < 32 ? lane * 4 + 512 : OOB;
    PIN(zw); PIN(hw); PIN(yo); PIN(st3);
#pragma nounroll
    for (int it = 0; it < SPIN * 4; ++it) { if (__builtin_amdgcn_s_memrealtime() - tstart >= T_POLLB) break; __builtin_amdgcn_s_sleep(4); }
    v4u yp[5];
    bool bd = true;
#pragma nounroll
    for (int it = 0; it < SPIN; ++it) {
      CBAR();
      if (bd) {
#pragma unroll
        for (int i = 0; i < 5; ++i) yp[i] = LD16(rws, yo, WS_YP + i * 512, SC1);
      }
      bd = umax(umax(umax(mx4(yp[0]), mx4(yp[1])), umax(mx4(yp[2]), mx4(yp[3]))), mx4(yp[4])) == SENT;
      if (none(bd)) break;
    }
#pragma unroll
    for (int i = 0; i < 5; ++i) {
      float at = red4(F4(yp[i]));
      at += dpp<ROR8>(at);
      *(LAS __bf16*)(L + zw + 256 * i) = (__bf16)fmaf(fa[i], at, fb[i]);
    }
    BARRIER();
    v4u af[5];
#pragma unroll
    for (int ks = 0; ks < 5; ++ks) af[ks] = *(const LAS v4u*)(zr + ks * 64);
    const v4u xa = *(const LAS v4u*)(L + S_X1 + lane * 16);
    const unsigned xb = *(const LAS unsigned*)(L + S_X1 + 1024 + lane * 4);
    SB();
    f32x4 d[4];
#pragma unroll
    for (int t = 0; t < 4; ++t) d[t] = f32x4{0.f, 0.f, 0.f, 0.f};
#pragma unroll
    for (int ks = 0; ks < 5; ++ks)
#pragma unroll
      for (int t = 0; t < 4; ++t) d[t] = mfma(af[ks], B1[ks][t], d[t]);
    const unsigned one2 = 0x3f803f80u;
    const float t1 = dot2(xb, one2, dot2(xa.x, one2, dot2(xa.y, one2, 0.f))) + dot2(xa.z, one2, dot2(xa.w, one2, 0.f));
    const float t2 = dot2(xb, xb, dot2(xa.x, xa.x, dot2(xa.y, xa.y, 0.f))) + dot2(xa.z, xa.z, dot2(xa.w, xa.w, 0.f));
    const float s12 = x32sum(rsum(sw16(t1, t2)));
    const float mean = U2F(__builtin_amdgcn_readlane(F2U(s12), 0)) * (1.f / D);
    const float var = fmaxf(U2F(__builtin_amdgcn_readlane(F2U(s12), 16)) * (1.f / D) - mean * mean, 0.f);
    const float rden = __builtin_amdgcn_rcpf(rbf(__builtin_amdgcn_sqrtf(rbf(rbf(var) + 1e-5f))));
    float tt = Ss - Cc * (rbf(mean) * rden);
    PIN(tt);
    const bool g1 = g & 1, g2 = g & 2;
#pragma unroll
    for (int t = 0; t < 4; ++t) PIN(d[t]);
    const f32x4 dd = SEL4(d);
    *(LAS __bf16*)(L + hw) = (__bf16)fmaxf(fmaf(FOLD4(dd), rden, tt), 0.f);
    BARRIER();
    const v4u ha0 = *(const LAS v4u*)(L + S_H + g * 16), ha1 = *(const LAS v4u*)(L + S_H + 64 + g * 16);
    f32x4 o[10];
#pragma unroll
    for (int t = 0; t < 10; ++t) o[t] = mfma(ha1, B2[1][t], mfma(ha0, B2[0][t], f32x4{0.f, 0.f, 0.f, 0.f}));
    float ov[10];
#pragma unroll
    for (int t = 0; t < 10; ++t) { ov[t] = o[t].x; PIN(ov[t]); }
    ST4(nosentf(g2 ? (g1 ? ov[3] : ov[2]) : (g1 ? ov[1] : ov[0])), rws, lane * 4, WS_FP + uc * 2560 + a * 640, SC1);
    ST4(nosentf(g2 ? (g1 ? ov[7] : ov[6]) : (g1 ? ov[5] : ov[4])), rws, lane * 4, WS_FP + uc * 2560 + a * 640 + 256, SC1);
    ST4(nosentf(g1 ? ov[9] : ov[8]), rws, st3, WS_FP + uc * 2560 + a * 640, SC1);
  }
  asm volatile(".Lpf_end:");
}

extern "C" void kernel_launch(void* const* d_in, const int* in_sizes, int n_in, void* d_out, int out_size, void* d_ws, size_t ws_size, hipStream_t stream) {
  fused_kernel<<<256, 512, 0, stream>>>((const u16*)d_in[1], (const u16*)d_in[2], (char*)d_ws, (const u16*)d_in[9], (const u16*)d_in[8], (const u16*)d_in[0],
      (const u16*)d_in[3], (const u16*)d_in[4], (const u16*)d_in[6], (const u16*)d_in[7], (u16*)d_out);
}
```

```cpp
#include <hip/hip_runtime.h>
#include <hip/hip_bf16.h>
#include <stdint.h>

typedef unsigned short u16;
typedef unsigned v4u __attribute__((ext_vector_type(4)));
typedef __bf16 bf16x2 __attribute__((ext_vector_type(2)));

constexpr int D = 640, HQ = 32, SK = 16384, DH = 128, W = 4096, DFF = 1728;
constexpr int NSPLIT = 8;

__device__ __forceinline__ float bf2f(u16 b) { return __uint_as_float((unsigned)b << 16); }
__device__ __forceinline__ float rbf(float f) { return (float)(__bf16)f; }
__device__ __forceinline__ float lo(unsigned u) { return __uint_as_float(u << 16); }
__device__ __forceinline__ float hi(unsigned u) { return __uint_as_float(u & 0xffff0000u); }

template <int K, int N>
__device__ __forceinline__ void gemv_block(const float* sx, const u16* Wt, float* sred, float& o0, float& o1) {
  const int tid = threadIdx.x, cp = tid & 31, ks = tid >> 5;
  const int n0 = blockIdx.x * 64 + cp * 2;
  float a0 = 0.f, a1 = 0.f;
  #pragma unroll 8
  for (int k = ks; k < K; k += 8) {
    unsigned w = *(const unsigned*)(Wt + (size_t)k * N + n0);
    float x = sx[k];
    a0 = fmaf(x, lo(w), a0); a1 = fmaf(x, hi(w), a1);
  }
  sred[(ks * 32 + cp) * 2] = a0; sred[(ks * 32 + cp) * 2 + 1] = a1;
  __syncthreads();
  o0 = 0.f; o1 = 0.f;
  if (tid < 32) { for (int s = 0; s < 8; ++s) { o0 += sred[(s * 32 + tid) * 2]; o1 += sred[(s * 32 + tid) * 2 + 1]; } }
}

__device__ __forceinline__ void block_ln(const float* sx_in, float* sred, float& mean, float& rstd) {
  const int tid = threadIdx.x;
  float s = 0.f;
  for (int i = tid; i < D; i += 256) s += sx_in[i];
  for (int o = 32; o; o >>= 1) s += __shfl_xor(s, o);
  if ((tid & 63) == 0) sred[tid >> 6] = s;
  __syncthreads();
  mean = (sred[0] + sred[1] + sred[2] + sred[3]) * (1.f / D);
  __syncthreads();
  float v = 0.f;
  for (int i = tid; i < D; i += 256) { float d = sx_in[i] - mean; v += d * d; }
  for (int o = 32; o; o >>= 1) v += __shfl_xor(v, o);
  if ((tid & 63) == 0) sred[tid >> 6] = v;
  __syncthreads();
  float var = (sred[0] + sred[1] + sred[2] + sred[3]) * (1.f / D);
  rstd = rbf(sqrtf(rbf(rbf(var) + 1e-5f)));
  mean = rbf(mean);
  __syncthreads();
}

__global__ void __launch_bounds__(256) k_mod(const u16* c, const u16* wmod, float* mod) {
  if (blockIdx.x == 0 && threadIdx.x == 0) *(unsigned long long*)(mod + 4000) = __builtin_amdgcn_s_memrealtime();
  __shared__ float sx[D]; __shared__ float sred[512];
  for (int i = threadIdx.x; i < D; i += 256) { float v = bf2f(c[i]); sx[i] = rbf(v / (1.f + __expf(-v))); }
  __syncthreads();
  float o0, o1; gemv_block<D, 6 * D>(sx, wmod, sred, o0, o1);
  if (threadIdx.x < 32) { int n0 = blockIdx.x * 64 + threadIdx.x * 2; mod[n0] = rbf(o0); mod[n0 + 1] = rbf(o1); }
}
__global__ void __launch_bounds__(256) k_q(const u16* x, const float* mod, const u16* wq, float* q) {
  __shared__ float sx[D]; __shared__ float sred[512];
  for (int i = threadIdx.x; i < D; i += 256) sx[i] = bf2f(x[i]);
  __syncthreads();
  float mean, rstd; block_ln(sx, sred, mean, rstd);
  for (int i = threadIdx.x; i < D; i += 256) sx[i] = rbf(rbf(rbf(rbf(sx[i] - mean) / rstd) * rbf(1.f + mod[D + i])) + mod[i]);
  __syncthreads();
  float o0, o1; gemv_block<D, HQ * DH>(sx, wq, sred, o0, o1);
  if (threadIdx.x < 32) { int n0 = blockIdx.x * 64 + threadIdx.x * 2; q[n0] = rbf(o0); q[n0 + 1] = rbf(o1); }
}
__global__ void __launch_bounds__(256) k_attn(const float* q, const u16* kc, const u16* vc, float* pml, float* po) {
  __shared__ float ss[512]; __shared__ float sred[16]; __shared__ float sacc[16][DH];
  const int s = blockIdx.x, h = blockIdx.y, tid = threadIdx.x, lane = tid & 63, wave = tid >> 6, r = lane >> 4, c = lane & 15;
  const u16* kb = kc + ((size_t)h * SK + (SK - W) + s * 512) * DH;
  const u16* vb = vc + ((size_t)h * SK + (SK - W) + s * 512) * DH;
  float qv[8];
  for (int j = 0; j < 8; ++j) qv[j] = q[h * DH + c * 8 + j];
  #pragma unroll 8
  for (int i = 0; i < 32; ++i) {
    int key = (i * 4 + wave) * 4 + r;
    v4u kk = *(const v4u*)(kb + (size_t)key * DH + c * 8);
    float d = lo(kk.x) * qv[0] + hi(kk.x) * qv[1] + lo(kk.y) * qv[2] + hi(kk.y) * qv[3] + lo(kk.z) * qv[4] + hi(kk.z) * qv[5] + lo(kk.w) * qv[6] + hi(kk.w) * qv[7];
    d += __shfl_xor(d, 1); d += __shfl_xor(d, 2); d += __shfl_xor(d, 4); d += __shfl_xor(d, 8);
    if (c == 0) ss[key] = rbf(rbf(d) * 0.08838834764831845f);
  }
  __syncthreads();
  float m = fmaxf(ss[tid], ss[tid + 256]);
  for (int o = 32; o; o >>= 1) m = fmaxf(m, __shfl_xor(m, o));
  if (lane == 0) sred[wave] = m;
  __syncthreads();
  m = fmaxf(fmaxf(sred[0], sred[1]), fmaxf(sred[2], sred[3]));
  float p0 = __expf(ss[tid] - m), p1 = __expf(ss[tid + 256] - m);
  ss[tid] = p0; ss[tid + 256] = p1;
  float l = p0 + p1;
  for (int o = 32; o; o >>= 1) l += __shfl_xor(l, o);
  if (lane == 0) sred[4 + wave] = l;
  __syncthreads();
  l = sred[4] + sred[5] + sred[6] + sred[7];
  float acc[8] = {0, 0, 0, 0, 0, 0, 0, 0};
  #pragma unroll 8
  for (int i = 0; i < 32; ++i) {
    int key = (i * 4 + wave) * 4 + r;
    v4u vv = *(const v4u*)(vb + (size_t)key * DH + c * 8);
    float p = ss[key];
    acc[0] += p * lo(vv.x); acc[1] += p * hi(vv.x); acc[2] += p * lo(vv.y); acc[3] += p * hi(vv.y);
    acc[4] += p * lo(vv.z); acc[5] += p * hi(vv.z); acc[6] += p * lo(vv.w); acc[7] += p * hi(vv.w);
  }
  for (int j = 0; j < 8; ++j) sacc[wave * 4 + r][c * 8 + j] = acc[j];
  __syncthreads();
  if (tid < DH) { float a = 0.f; for (int g = 0; g < 16; ++g) a += sacc[g][tid]; po[(h * NSPLIT + s) * DH + tid] = a; }
  if (tid == 0) { pml[(h * NSPLIT + s) * 2] = m; pml[(h * NSPLIT + s) * 2 + 1] = l; }
}
__global__ void __launch_bounds__(256) k_wo(const float* pml, const float* po, const u16* wo, float* part) {
  __shared__ float sx[DH]; __shared__ float sred[512];
  const int h = blockIdx.y, tid = threadIdx.x;
  if (tid < DH) {
    float M = -INFINITY; for (int s = 0; s < NSPLIT; ++s) M = fmaxf(M, pml[(h * NSPLIT + s) * 2]);
    float L = 0.f, o = 0.f;
    for (int s = 0; s < NSPLIT; ++s) { float w = __expf(pml[(h * NSPLIT + s) * 2] - M); L += w * pml[(h * NSPLIT + s) * 2 + 1]; o += w * po[(h * NSPLIT + s) * DH + tid]; }
    sx[tid] = rbf(o / L);
  }
  __syncthreads();
  float o0, o1; gemv_block<DH, D>(sx, wo + (size_t)h * DH * D, sred, o0, o1);
  if (tid < 32) { int n0 = blockIdx.x * 64 + tid * 2; part[h * D + n0] = o0; part[h * D + n0 + 1] = o1; }
}
__global__ void __launch_bounds__(256) k_w1(const u16* x, const float* mod, const float* part, const u16* w1, float* hid, float* x1out) {
  __shared__ float sx[D]; __shared__ float sred[512];
  for (int i = threadIdx.x; i < D; i += 256) { float a = 0.f; for (int h = 0; h < HQ; ++h) a += part[h * D + i]; float v = rbf(bf2f(x[i]) + rbf(mod[2 * D + i] * rbf(a))); sx[i] = v; if (blockIdx.x == 0) x1out[i] = v; }
  __syncthreads();
  float mean, rstd; block_ln(sx, sred, mean, rstd);
  for (int i = threadIdx.x; i < D; i += 256) sx[i] = rbf(rbf(rbf(rbf(sx[i] - mean) / rstd) * rbf(1.f + mod[4 * D + i])) + mod[3 * D + i]);
  __syncthreads();
  float o0, o1; gemv_block<D, DFF>(sx, w1, sred, o0, o1);
  if (threadIdx.x < 32) { int n0 = blockIdx.x * 64 + threadIdx.x * 2; hid[n0] = fmaxf(rbf(o0), 0.f); hid[n0 + 1] = fmaxf(rbf(o1), 0.f); }
}
__global__ void __launch_bounds__(256) k_w2(const float* hid, const float* x1, const float* mod, const u16* w2, __hip_bfloat16* out) {
  __shared__ float sx[DFF]; __shared__ float sred[512];
  for (int i = threadIdx.x; i < DFF; i += 256) sx[i] = hid[i];
  __syncthreads();
  float o0, o1; gemv_block<DFF, D>(sx, w2, sred, o0, o1);
  if (threadIdx.x < 32) { int n0 = blockIdx.x * 64 + threadIdx.x * 2;
    out[n0] = __float2bfloat16(x1[n0] + rbf(mod[5 * D + n0] * rbf(o0))); out[n0 + 1] = __float2bfloat16(x1[n0 + 1] + rbf(mod[5 * D + n0 + 1] * rbf(o1))); }
}

extern "C" void kernel_launch(void* const* d_in, const int* in_sizes, int n_in, void* d_out, int out_size, void* d_ws, size_t ws_size, hipStream_t stream) {
  const u16* x = (const u16*)d_in[0]; const u16* kc = (const u16*)d_in[1]; const u16* vc = (const u16*)d_in[2];
  const u16* wq = (const u16*)d_in[3]; const u16* wo = (const u16*)d_in[4]; const u16* w1 = (const u16*)d_in[6]; const u16* w2 = (const u16*)d_in[7];
  const u16* c = (const u16*)d_in[8]; const u16* wmod = (const u16*)d_in[9];
  float* ws = (float*)d_ws;
  float* mod = ws; float* q = ws + 4096; float* pml = ws + 8192; float* po = ws + 12288; float* part = po + HQ * NSPLIT * DH; float* hid = part + HQ * D; float* x1 = hid + 2048;
  k_mod<<<6 * D / 64, 256, 0, stream>>>(c, wmod, mod);
  k_q<<<HQ * DH / 64, 256, 0, stream>>>(x, mod, wq, q);
  k_attn<<<dim3(NSPLIT, HQ), 256, 0, stream>>>(q, kc, vc, pml, po);
  k_wo<<<dim3(D / 64, HQ), 256, 0, stream>>>(pml, po, wo, part);
  k_w1<<<DFF / 64, 256, 0, stream>>>(x, mod, part, w1, hid, x1);
  k_w2<<<D / 64, 256, 0, stream>>>(hid, x1, mod, w2, (__hip_bfloat16*)d_out);
}
```

```cpp
#include <hip/hip_runtime.h>
#include <hip/hip_bf16.h>
#include <stdint.h>

typedef unsigned short u16;
typedef unsigned v4u __attribute__((ext_vector_type(4)));
typedef unsigned v2u __attribute__((ext_vector_type(2)));
typedef float f32x4 __attribute__((ext_vector_type(4)));
typedef float f32x2 __attribute__((ext_vector_type(2)));
typedef __bf16 bf16x8 __attribute__((ext_vector_type(8)));
typedef __bf16 bf16x2 __attribute__((ext_vector_type(2)));
#define LAS __attribute__((address_space(3)))
#define DI __device__ __forceinline__
constexpr unsigned SENT = 0xFFFFFFFFu;
constexpr int SPIN = 200000;
constexpr int OOB = 0x7fffff00;
DI float bflo(unsigned u) { return __uint_as_float(u << 16); }
DI float bfhi(unsigned u) { return __uint_as_float(u & 0xffff0000u); }
DI unsigned cvtpk(float lo, float hi) { bf16x2 v = {(__bf16)lo, (__bf16)hi}; return __builtin_bit_cast(unsigned, v); }
DI float rbf(float f) { return (float)(__bf16)f; }
DI f32x4 mfma(v4u a, v4u b, f32x4 c) { return __builtin_amdgcn_mfma_f32_16x16x32_bf16(__builtin_bit_cast(bf16x8, a), __builtin_bit_cast(bf16x8, b), c, 0, 0, 0); }
DI unsigned umax(unsigned a, unsigned b) { return a > b ? a : b; }
DI unsigned nosentf(float f) { const unsigned u = __float_as_uint(f); return u < 0xFFFFFFFEu ? u : 0xFFFFFFFEu; }
DI unsigned mx4(v4u v) { return umax(umax(v.x, v.y), umax(v.z, v.w)); }
DI bool none(bool p) { return __builtin_amdgcn_ballot_w64(p) == 0ull; }
#define CBAR() asm volatile("" ::: "memory")
#define LGKM0() asm volatile("s_waitcnt lgkmcnt(0)" ::: "memory")
#define SB() __builtin_amdgcn_sched_barrier(0)
#define PIN(x) asm volatile("" : "+v"(x))
#define F4(v) __builtin_bit_cast(f32x4, v)
#define U2F(u) __uint_as_float(u)
#define F2U(f) __float_as_uint(f)
template <int CTRL> DI float dpp(float x) { return __builtin_bit_cast(float, __builtin_amdgcn_mov_dpp(__builtin_bit_cast(int, x), CTRL, 0xf, 0xf, true)); }
constexpr int XOR1 = 0xB1, XOR2 = 0x4E, XOR7 = 0x141, ROR8 = 0x128, ROR4 = 0x124, BCAST = 0x150;
DI float sw16(float a, float b) { auto s = __builtin_amdgcn_permlane16_swap(F2U(a), F2U(b), false, false); return U2F(s[0]) + U2F(s[1]); }
DI float sw32(float a, float b) { auto s = __builtin_amdgcn_permlane32_swap(F2U(a), F2U(b), false, false); return U2F(s[0]) + U2F(s[1]); }
DI float x16sum(float x) { return sw16(x, x); }
DI float x32sum(float x) { return sw32(x, x); }
DI float x16max(float x) { auto s = __builtin_amdgcn_permlane16_swap(F2U(x), F2U(x), false, false); return fmaxf(U2F(s[0]), U2F(s[1])); }
DI float x32max(float x) { auto s = __builtin_amdgcn_permlane32_swap(F2U(x), F2U(x), false, false); return fmaxf(U2F(s[0]), U2F(s[1])); }
DI float rsum(float x) { x += dpp<XOR1>(x); x += dpp<XOR2>(x); x += dpp<XOR7>(x); x += dpp<ROR8>(x); return x; }
DI float wsum(float x) { return x32sum(x16sum(rsum(x))); }
DI float wmax(float x) { x = fmaxf(x, dpp<XOR1>(x)); x = fmaxf(x, dpp<XOR2>(x)); x = fmaxf(x, dpp<XOR7>(x)); x = fmaxf(x, dpp<ROR8>(x)); return x32max(x16max(x)); }
typedef __amdgpu_buffer_rsrc_t rsrc_t;
DI rsrc_t mkrs(const void* p, int bytes) { return __builtin_amdgcn_make_buffer_rsrc((void*)p, 0, bytes, 0x00020000); }
#define LD16(r, vo, so, aux) __builtin_amdgcn_raw_buffer_load_b128((r), (vo), (so), (aux))
#define LD8(r, vo, so, aux) __builtin_amdgcn_raw_buffer_load_b64((r), (vo), (so), (aux))
#define LD4(r, vo, so, aux) __builtin_amdgcn_raw_buffer_load_b32((r), (vo), (so), (aux))
#define ST16(v, r, vo, so, aux) do { __builtin_amdgcn_raw_buffer_store_b128((v), (r), (vo), (so), (aux)); __builtin_amdgcn_sched_barrier(0); asm volatile("s_nop 1" ::: "memory"); __builtin_amdgcn_sched_barrier(0); } while (0)
#define ST8(v, r, vo, so, aux) __builtin_amdgcn_raw_buffer_store_b64((v), (r), (vo), (so), (aux))
#define ST4(v, r, vo, so, aux) __builtin_amdgcn_raw_buffer_store_b32((v), (r), (vo), (so), (aux))
constexpr int NT = 2, SC1 = 16;
DI void lds_wait(volatile LAS unsigned* f, unsigned want) {
#pragma nounroll
  for (int i = 0; i < SPIN * 8; ++i) { if (__builtin_amdgcn_readfirstlane(*f) >= want) break; __builtin_amdgcn_s_sleep(1); }
  CBAR();
}
DI void lds_arrive(LAS unsigned* c, int lane) {
  LGKM0();
  if (lane == 0) __hip_atomic_fetch_add(c, 1u, __ATOMIC_RELAXED, __HIP_MEMORY_SCOPE_WORKGROUP);
  CBAR();
}
#define BARRIER() do { LGKM0(); __builtin_amdgcn_s_barrier(); CBAR(); } while (0)
#define VMW(n) asm volatile("s_waitcnt vmcnt(%0)" :: "n"(n) : "memory")
DI v4u selfrag(unsigned p, const v4u& mk) { return v4u{p & mk.x, p & mk.y, p & mk.z, p & mk.w}; }
DI v4u selmask(int c) { const int r7 = c & 7; const unsigned hm = (r7 & 1) ? 0xffff0000u : 0x0000ffffu;
  return v4u{(r7 >> 1) == 0 ? hm : 0u, (r7 >> 1) == 1 ? hm : 0u, (r7 >> 1) == 2 ? hm : 0u, (r7 >> 1) == 3 ? hm : 0u}; }
DI unsigned permlo(unsigned a, unsigned b) { return __builtin_amdgcn_perm(b, a, 0x05040100u); }
DI unsigned permhi(unsigned a, unsigned b) { return __builtin_amdgcn_perm(b, a, 0x07060302u); }
template <int N> DI unsigned badtags(const v4u (&u)[N], unsigned elo, unsigned ehi) {
  unsigned bad = 0;
#pragma unroll
  for (int k = 0; k < N; ++k) bad |= (u[k].y ^ elo) | (u[k].w ^ ehi);
  return bad;
}


constexpr int D = 640, HQ = 32, SK = 16384, DH = 128, WIN = 4096, DFF = 1728;
#define KV_WIN 8
#define KV_W1 6
#define KV_N1 4
#define AW_LO 5
#define AW_MID 6
#define AW_T44 690
#define AW_SL 131
#define AW_D 40
#define PV_AT 36
#define SV_WQ 6
#define SV_W 5
#define SV_WC 3
#define SV_WD 8
#define T_POLL1 370
#define T_POLLQ 790
#define T_POLLB 1000
#define T_D 125
#define AW_SET(n_) do { const int lag_ = (int)(__builtin_amdgcn_s_memrealtime() - tstart) - (AW_T44 + ((n_) - 44) * AW_SL / 10); lvl = lag_ < -AW_D / 2 ? 0 : (lag_ < AW_D / 2 ? 1 : 2); } while (0)
#define AW() asm volatile("s_waitcnt vmcnt(%1)\n\ts_cmp_eq_u32 %0, 2\n\ts_cbranch_scc1 1f\n\ts_waitcnt vmcnt(%2)\n\ts_cmp_eq_u32 %0, 1\n\ts_cbranch_scc1 1f\n\ts_waitcnt vmcnt(%3)\n1:" \
                          :: "s"(lvl), "n"(KV_WIN), "n"(AW_MID), "n"(AW_LO) : "memory", "scc")
#define VMW2(sel_, hi_, lo_) asm volatile("s_waitcnt vmcnt(%1)\n\ts_cmp_lt_u32 %0, 3\n\ts_cbranch_scc1 1f\n\ts_waitcnt vmcnt(%2)\n1:" :: "s"(sel_), "n"(hi_), "n"(lo_) : "memory", "scc")
constexpr int WS_EPOCH = 0;
constexpr int WS_MP = 4096;
constexpr int WS_QF = WS_MP + 61440;
constexpr int WS_FL = WS_QF + 16384;
constexpr int WS_OP = WS_FL + 8192;
constexpr int WS_YP = WS_OP + 122880;
constexpr int WS_FP = WS_YP + 20480;
constexpr int WS_BYTES = WS_FP + 69120;
constexpr int L_K = 0;
constexpr int L_P = 147456;
constexpr int L_Q = L_P + 6656;
constexpr int L_OM = L_Q + 256;
constexpr int L_F = 163584;
constexpr int L_END = L_F + 256;
enum { F_KA = 0  , F_KB = 4  , F_FREE = 8  , F_QRDY = 12, F_CML = 13, F_CP = 14, F_M = 16  , F_L = 20  , F_MCU = 24, F_LCU = 25 };
constexpr int S_W1 = 0;
constexpr int S_W2 = 81920;
constexpr int S_MB = 131072;
constexpr int S_S = S_MB + 4096;
constexpr int S_Y = S_S + 2560;
constexpr int S_Z = S_Y + 2048;
constexpr int S_X1 = S_Z + 2560;
constexpr int S_H = S_X1 + 1280;
constexpr int S_MV = S_H + 128;
constexpr int S_HT = S_MV + 7680;
constexpr int S_QM = 0;
constexpr int S_ST = S_HT + 2560;
constexpr int S_O = S_ST + 64;
static_assert(SV_WC <= SV_WD && SV_WD < 12, "");
enum { G_M1 = 0, G_M2 = 1, G_W1 = 2, G_W2 = 3, G_CV = 4, G_P1 = 5, G_P2 = 6, G_P3 = 7, G_Y = 8, G_H = 9, G_ARM = 10, G_Q = 11 };
static_assert(S_O + 7616 <= L_F && L_OM + 4096 <= L_F && L_END <= 163840, "");

#define FOLD4(d_) (((d_).x + dpp<0x104>((d_).y)) + (dpp<0x108>((d_).z) + dpp<0x10C>((d_).w)))
DI float dot2(unsigned a, unsigned b, float acc) { return __builtin_amdgcn_fdot2_f32_bf16(__builtin_bit_cast(bf16x2, a), __builtin_bit_cast(bf16x2, b), acc, false); }
#define DMA(m0_, vo_, srd_, so_, pol_) do { unsigned keep_; \
    asm volatile("s_mov_b32 %0, m0\n\ts_mov_b32 m0, %1\n\ts_nop 0\n\tbuffer_load_dwordx4 %2, %3, %4 offen " pol_ " lds\n\ts_mov_b32 m0, %0" \
                 : "=&s"(keep_) : "s"(m0_), "v"(vo_), "s"(srd_), "s"(so_) : "memory"); } while (0)

DI v2u ld_epoch(rsrc_t rws, int lane) { return LD8(rws, lane == 0 ? 0 : OOB, WS_EPOCH, SC1); }
#define EPOCH_TAG(ep_, lo_, hi_) do { lo_ = (unsigned)__builtin_amdgcn_readfirstlane((ep_).x) + 1u; hi_ = (unsigned)__builtin_amdgcn_readfirstlane((ep_).y) + 1u; } while (0)
__global__ void __launch_bounds__(512, 2) fused_kernel(const u16* kc, const u16* vc, char* ws, const u16* wmod, const u16* cvec, const u16* x,
    const u16* wq, const u16* wo, const u16* w1, const u16* w2, u16* out) {
  __shared__ __attribute__((aligned(16))) char smem[L_END];
  LAS char* L = (LAS char*)smem;
  const int tid = threadIdx.x, lane = tid & 63, wave = __builtin_amdgcn_readfirstlane(tid >> 6);
  const int b = blockIdx.x, xg = b & 7, r = b >> 3;
  const int g = lane >> 4, c = lane & 15;
  const unsigned lds0 = (unsigned)(uintptr_t)L;
  const unsigned long long tstart = __builtin_amdgcn_s_memrealtime();
  unsigned long long pc0; unsigned pclen;
  asm volatile("s_getpc_b64 %0\n.Lpf_here:\n\ts_mov_b32 %1, .Lpf_end-.Lpf_here\n\ts_max_i32 %1, %1, .Lpf_end2-.Lpf_here" : "=s"(pc0), "=s"(pclen) :: "scc");

  if (__builtin_expect(r < 24, 1)) {
    volatile LAS unsigned* FL = (volatile LAS unsigned*)(L + L_F);
    const int hd = xg * 4 + (r & 3), sp = r >> 2, j = wave & 3;
    const int nr = sp < 4 ? 43 : 42, r0 = sp * 43 - (sp == 5 ? 1 : 0);
    const size_t kvoff = ((size_t)hd * SK + (SK - WIN) + r0 * 16) * DH;
    if (__builtin_expect(wave < 4, 1)) {
      unsigned pf[3];
      {
        const int t = 95 - (r * 4 + j);
        const unsigned n = t * 17 + lane, u5 = (n * 52429u) >> 24, um = xg * 5 + u5;
        pf[0] = LD4(mkrs(wmod, D * 6 * D * 2), (lane < 17 && n < 1600u) ? ((um & 1u) * 320u + n - u5 * 320u) * 7680u + (um >> 1) * 128u : OOB, 0, 0);
        const unsigned m = t * 54 + lane;
        pf[1] = LD4(mkrs(wq, D * HQ * DH * 2), lane < 54 ? (m >> 3) * 8192u + (m & 7u) * 128u : OOB, xg * 1024, 0);
        pf[2] = LD4(mkrs((const void*)pc0, (int)pclen), lane < 3 ? (t * 3 + lane) * 128 : OOB, 0, 0);
        SB(); CBAR();
      }
      const unsigned long long ka = (unsigned long long)(kc + kvoff);
      const v4u srdK = {(unsigned)ka, (unsigned)(ka >> 32), (unsigned)nr * 4096u, 0x00020000u};
      int vo[4];
#pragma unroll
      for (int q = 0; q < 4; ++q) vo[q] = g * 256 + ((c ^ (4 * q + g)) * 16);
      const unsigned lbase = lds0 + L_K + j * 36864;
      int lvl = 1;
      asm volatile("s_nop 4" ::: "memory");
#pragma nounroll
      for (int i4 = 0; i4 < 11; ++i4) {
        if (i4 == 9) lds_wait(FL + F_FREE + j, 1);
        const unsigned mb = lbase + (i4 < 9 ? i4 : i4 - 9) * 4096, sb = i4 * 16384 + j * 1024;
#pragma unroll
        for (int q = 0; q < 4; ++q) { DMA(mb + q * 1024, vo[q], srdK, sb + q * 4096, "nt"); if (q == 0 && i4 == 0) __builtin_amdgcn_s_barrier();   if (i4 < KV_N1) VMW(KV_W1); else AW(); }
        AW_SET(i4 * 4 + 4);
        if (i4 == (8 + KV_WIN + 3) / 4 - 1) FL[F_KA + j] = 1u;
      }
      const rsrc_t rws = mkrs(ws, WS_BYTES);
      const rsrc_t rv = mkrs(vc + kvoff, nr * 4096);
      const v2u ep = ld_epoch(rws, lane);
      v4u V[43];
#pragma unroll
      for (int i = 0; i <= PV_AT; ++i) {
        V[i] = LD16(rv, lane * 16, i * 4096 + j * 1024, NT);
        AW();
        if ((i & 3) == 3) AW_SET(45 + i);
        if (i == KV_WIN - 1) { FL[F_KB + j] = 1u; }
        SB();
      }
#pragma unroll
      for (int i = 0; i < 3; ++i) asm volatile("" :: "v"(pf[i]));
      unsigned elo, ehi;
      EPOCH_TAG(ep, elo, ehi);
      const v4u mk = selmask(c);
      int sto = lane < 32 ? sp * 512 + c * 32 + g * 16 : (lane == 32 ? 3072 + sp * 128 : OOB);
      PIN(sto);
      lds_wait(FL + F_CP, 4);
      const LAS v4u* pt = (const LAS v4u*)(L + L_P + (((j * 2 + (c >> 3)) * 4 + g) * 52) * 4);
      v4u p4[11];
#pragma unroll
      for (int i4 = 0; i4 < 11; ++i4) p4[i4] = pt[i4];
      unsigned mcu = FL[F_MCU], lcu = FL[F_LCU];
      SB();
      f32x4 acc = {0.f, 0.f, 0.f, 0.f};
      constexpr int PER = (43 - KV_WIN - 1) / (42 - PV_AT);
#pragma unroll
      for (int i = PV_AT + 1; i < 43; ++i) {
        V[i] = LD16(rv, lane * 16, i * 4096 + j * 1024, NT);
        SB();
#pragma unroll
        for (int u = 0; u < PER; ++u) { const int n = (i - PV_AT - 1) * PER + u; acc = mfma(selfrag(p4[n >> 2][n & 3], mk), V[n], acc); }
        SB();
        AW();
        SB();
      }
      PIN(mcu); PIN(lcu);
#pragma unroll
      for (int n = (42 - PV_AT) * PER; n < 43; ++n) acc = mfma(selfrag(p4[n >> 2][n & 3], mk), V[n], acc);
      ((LAS f32x4*)(L + L_OM))[j * 64 + lane] = acc;
      BARRIER();
      if (j == 0) {
        const LAS f32x4* om = (const LAS f32x4*)(L + L_OM) + (lane & 31);
        const f32x4 o = ((om[0] + om[32]) + (om[64] + om[96])) + ((om[128] + om[160]) + (om[192] + om[224]));
        const bool ml = lane >= 32;
        const v4u st = {ml ? mcu : cvtpk(o.x, o.y), elo, ml ? lcu : cvtpk(o.z, o.w), ehi};
        ST16(st, rws, sto, WS_OP + hd * 3840, SC1);
      }
      return;
    }
    const rsrc_t rws = mkrs(ws, WS_BYTES);
    v2u ep = ld_epoch(rws, lane);
    FL[lane] = 0u;
    BARRIER();
    const LAS char* kb = L + L_K + j * 36864 + c * 256;
    v4u kf[11][4];
    lds_wait(FL + F_KA + j, 1);
#pragma unroll
    for (int t = 0; t < 2; ++t)
#pragma unroll
      for (int ks = 0; ks < 4; ++ks) kf[t][ks] = *(const LAS v4u*)(kb + t * 4096 + (((4 * ks + g) ^ c) * 16));
    SB();
    LGKM0();
    FL[F_FREE + j] = 1u;
    if (j == 0) {
      unsigned elo, ehi;
      EPOCH_TAG(ep, elo, ehi);
#pragma nounroll
      for (int it = 0; it < SPIN * 4; ++it) { if (__builtin_amdgcn_s_memrealtime() - tstart >= T_POLLQ) break; __builtin_amdgcn_s_sleep(2); }
      v4u qu[1];
      bool bd = lane < 32;
#pragma nounroll
      for (int it = 0; it < SPIN; ++it) {
        CBAR();
        if (bd) { qu[0] = LD16(rws, lane * 16, WS_QF + hd * 512, SC1); bd = badtags<1>(qu, elo, ehi) != 0u; }
        if (none(bd)) break;
      }
      if (lane < 32) ((LAS v2u*)(L + L_Q))[lane] = v2u{qu[0].x, qu[0].z};
      LGKM0();
      FL[F_QRDY] = 1u;
    }
    lds_wait(FL + F_KB + j, 1);
#pragma unroll
    for (int t = 2; t < 11; ++t)
#pragma unroll
      for (int ks = 0; ks < 4; ++ks) kf[t][ks] = *(const LAS v4u*)(kb + (t < 9 ? t : t - 9) * 4096 + (((4 * ks + g) ^ c) * 16));
    SB();
    lds_wait(FL + F_QRDY, 1);
    v4u qa[4];
#pragma unroll
    for (int ks = 0; ks < 4; ++ks) qa[ks] = *(const LAS v4u*)(L + L_Q + (4 * ks + g) * 16);
    float sc[11];
#pragma unroll
    for (int t = 0; t < 11; ++t) {
      f32x4 d = {0.f, 0.f, 0.f, 0.f};
#pragma unroll
      for (int ks = 0; ks < 4; ++ks) d = mfma(qa[ks], kf[t][ks], d);
      sc[t] = d.x;
    }
#pragma unroll
    for (int t = 0; t < 11; ++t) PIN(sc[t]);
    const bool g1 = g & 1, g2 = g & 2;
    float s0 = g2 ? (g1 ? sc[3] : sc[2]) : (g1 ? sc[1] : sc[0]);
    float s1 = g2 ? (g1 ? sc[7] : sc[6]) : (g1 ? sc[5] : sc[4]);
    float s2 = g2 ? sc[10] : (g1 ? sc[9] : sc[8]);
    s0 = rbf(rbf(s0) * 0.08838834764831845f); s1 = rbf(rbf(s1) * 0.08838834764831845f); s2 = rbf(rbf(s2) * 0.08838834764831845f);
    const bool v2 = 128 + lane < 4 * nr;
    if (!v2) s2 = -INFINITY;
    const float m = wmax(fmaxf(fmaxf(s0, s1), s2));
    float p0 = __expf(s0 - m), p1 = __expf(s1 - m), p2 = v2 ? __expf(s2 - m) : 0.f;
    const float l = wsum((p0 + p1) + p2);
    if (lane == 0) { FL[F_M + j] = F2U(m); FL[F_L + j] = F2U(l); }
    lds_arrive((LAS unsigned*)(L + L_F) + F_CML, lane);
    lds_wait(FL + F_CML, 4);
    {
      const f32x4 m4 = *(const LAS f32x4*)(L + L_F + F_M * 4), l4 = *(const LAS f32x4*)(L + L_F + F_L * 4);
      const float mcu = fmaxf(fmaxf(m4.x, m4.y), fmaxf(m4.z, m4.w));
      const float lcu = (l4.x * __expf(m4.x - mcu) + l4.y * __expf(m4.y - mcu)) + (l4.z * __expf(m4.z - mcu) + l4.w * __expf(m4.w - mcu));
      const float f = __expf(m - mcu) / lcu;
      p0 *= f; p1 *= f; p2 *= f;
      if (lane == 0 && j == 0) { FL[F_MCU] = F2U(mcu); FL[F_LCU] = F2U(lcu); }
    }
    const unsigned h0 = cvtpk(p0, p0), h1 = cvtpk(p1, p1), h2 = cvtpk(p2, p2);
    const float e0 = p0 - bflo(h0), e1 = p1 - bflo(h1), e2 = p2 - bflo(h2);
    LAS unsigned* pw = (LAS unsigned*)(L + L_P) + (j * 8 + (lane & 3)) * 52 + (lane >> 2);
    pw[0] = h0; pw[16] = h1; pw[32] = h2; pw[208] = cvtpk(e0, e0); pw[224] = cvtpk(e1, e1); pw[240] = cvtpk(e2, e2);
    lds_arrive((LAS unsigned*)(L + L_F) + F_CP, lane);
    return;
  }

  const int a = wave & 3, k = r - 24;
  const bool role_b = k < 5, role_c = k >= 5 || (k == 0 && xg < 3), role_m = !(k == 0 && xg < 4);
  const int uc = k >= 5 ? xg * 3 + k - 5 : 24 + xg;
  const int chi = c & 7, rho = c >> 3;
  const rsrc_t rws = mkrs(ws, WS_BYTES);
  const rsrc_t rx = mkrs(x, D * 2);
  volatile LAS unsigned* FL = (volatile LAS unsigned*)(L + L_F);
  LAS unsigned* FC = (LAS unsigned*)(L + L_F);
  auto red4 = [&](f32x4 t) { return sw32(sw16(t.x, t.y), sw16(t.z, t.w)); };
  unsigned elo, ehi;
  const bool role_q = k >= 1 && k <= 4;

  if (wave < 4) {
    const int hb = xg * 4 + a;
    const int dual = b - 192;
    const v2u ep = ld_epoch(rws, lane);
    __builtin_amdgcn_s_barrier();
    EPOCH_TAG(ep, elo, ehi);
    if (role_q) {
      v4u WQ[40];
      const rsrc_t rq = mkrs(wq, D * HQ * DH * 2);
#pragma unroll
      for (int i = 0; i < 40; ++i) { WQ[i] = LD16(rq, (40 * g) * 8192 + c * 16, (160 * a + i) * 8192 + (xg * 4 + k - 1) * 256, 0); VMW(SV_WQ); SB(); }
      const v4u mk = selmask(c);
      lds_wait(FL + G_H, 4);
      const LAS v4u* ht = (const LAS v4u*)(L + S_HT + (160 * a + 40 * g) * 4);
      f32x4 acc = {0.f, 0.f, 0.f, 0.f}, acc2 = {0.f, 0.f, 0.f, 0.f};
      v4u h4[10];
#pragma unroll
      for (int i = 0; i < 10; ++i) h4[i] = ht[i];
      SB();
#pragma unroll
      for (int i = 0; i < 40; i += 2) { acc = mfma(selfrag(h4[i >> 2][i & 3], mk), WQ[i], acc); acc2 = mfma(selfrag(h4[i >> 2][(i & 3) + 1], mk), WQ[i + 1], acc2); }
      if (lane < 32) ((LAS f32x4*)(L + S_QM))[a * 32 + c * 2 + g] = acc + acc2;
      lds_arrive(FC + G_Q, lane);
      lds_wait(FL + G_Q, 4);
      if (a == 0) {
        const LAS f32x2* mb = (const LAS f32x2*)(L + S_QM) + lane;
        const f32x2 qv = (mb[0] + mb[64]) + (mb[128] + mb[192]);
        const unsigned qp = cvtpk(qv.x, qv.y), qn = F2U(dpp<XOR1>(U2F(qp)));
        ST16((v4u{qp, elo, qn, ehi}), rws, !(lane & 1) ? lane * 8 : OOB, WS_QF + (xg * 4 + k - 1) * 512, SC1);
      } else __builtin_amdgcn_s_sleep(6);
    } else if (k == 5) lds_wait(FL + G_M1, 5);
    v4u WB[32];
    if (role_b) {
      const rsrc_t ro = mkrs(wo, HQ * DH * D * 2);
#pragma unroll
      for (int i = 0; i < 32; ++i) {
        WB[i] = LD16(ro, (8 * g) * 1280 + c * 16, (hb * 128 + 32 * (i >> 3) + (i & 7)) * 1280 + k * 256, NT);
        VMW2(dual, SV_WD, SV_W);
        SB();
      }
    }
    if (role_c) {
      const unsigned long long wa = (unsigned long long)w1, wb = (unsigned long long)(w2 + (size_t)uc * 64 * D);
      const v4u srd1 = {(unsigned)wa, (unsigned)(wa >> 32), (unsigned)(D * DFF * 2), 0x00020000u};
      const v4u srd2 = {(unsigned)wb, (unsigned)(wb >> 32), 81920u, 0x00020000u};
      const int f0 = (g & 1) | ((a & 1) << 2);
      const int voa = (lane >> 3) * 3456 + (((lane & 7) ^ f0) * 16), vob = (lane >> 3) * 3456 + (((lane & 7) ^ f0 ^ 2) * 16);
      asm volatile("s_nop 4" ::: "memory");
#pragma nounroll
      for (int i = 0; i < 20; i += 2) {
        const unsigned m0v = lds0 + S_W1 + (a * 20 + i) * 1024, so = (a * 160 + i * 8) * 3456 + uc * 128;
        DMA(m0v, voa, srd1, so, "nt"); VMW2(dual, SV_WD, SV_WC); DMA(m0v + 1024, vob, srd1, so + 8 * 3456, "nt"); VMW2(dual, SV_WD, SV_WC);
      }
#pragma nounroll
      for (int i = 0; i < 20; ++i) {
        const int p = 4 * i + a, row = ((4 * p + g) * 205) >> 10;
        const int vo2 = g * 256 + ((c ^ (((row & 3) | ((row >> 1) & 4)) << 1)) * 16);
        if (i == 12) lds_wait(FL + G_CV, 4);
        const unsigned m0v = lds0 + (i < 12 ? S_W2 + p * 1024 : S_W1 + (p - 48) * 1024), so = p * 1024;
        DMA(m0v, vo2, srd2, so, "nt"); VMW2(dual, SV_WD, SV_WC);
        if (i == SV_WD) lds_arrive(FC + G_W1, lane);
      }
      VMW(0);
      lds_arrive(FC + G_W2, lane);
    }
    if (!role_b) return;
    v4u BF[4][8];
#pragma unroll
    for (int hf = 0; hf < 4; ++hf) {
#pragma unroll
      for (int m = 0; m < 4; ++m)
#pragma unroll
        for (int d = 0; d < 4; ++d) { BF[hf][2 * d][m] = permlo(WB[hf * 8 + 2 * m][d], WB[hf * 8 + 2 * m + 1][d]); BF[hf][2 * d + 1][m] = permhi(WB[hf * 8 + 2 * m][d], WB[hf * 8 + 2 * m + 1][d]); }
#pragma unroll
      for (int t = 0; t < 8; ++t) PIN(BF[hf][t]);
    }
    const bool role_d = (a == 1 && k > 0) || (a == 2 && k == 1);
    const int v = xg * 5 + (a == 1 ? k : 0);
    const int q4 = lane & 3, part = lane >> 2;
    float dx = 0.f, dga = 0.f, dgb = 0.f;
    if (role_d) {
      const unsigned xv = LD4(rx, q4 * 8 + (g >> 1) * 4, v * 32, 0);
      v4u mm[2];
      bool bd = true;
#pragma nounroll
      for (int it = 0; it < SPIN; ++it) {
        CBAR();
        if (bd) {
          mm[0] = LD16(rws, ((lane >> 3) & 1) * 30720 + ((lane >> 4) & 1) * 15360 + (lane & 7) * 16, WS_MP + 10240 + v * 128, SC1);
          mm[1] = LD16(rws, lane * 128, WS_FL, SC1);
          bd = badtags<2>(mm, elo, ehi) != 0u;
        }
        if (none(bd)) break;
        __builtin_amdgcn_s_sleep(16);
      }
      const bool odd = g & 1;
      dx = odd ? bfhi(xv) : bflo(xv);
      const float e0 = U2F(mm[0].x) + dpp<ROR8>(U2F(mm[0].x)), e1 = U2F(mm[0].z) + dpp<ROR8>(U2F(mm[0].z));
      const int src = q4 * 2 + (g >> 1);
      const float a0 = __shfl(e0, src), a1 = __shfl(e1, src), b0 = __shfl(e0, 16 + src), b1 = __shfl(e1, 16 + src);
      dga = rbf(odd ? a1 : a0); dgb = rbf(odd ? b1 : b0);
      PIN(dx); PIN(dga); PIN(dgb);
    }
    lds_wait(FL + G_ARM, 1);
#pragma nounroll
    for (int it = 0; it < SPIN * 4; ++it) { if (__builtin_amdgcn_s_memrealtime() - tstart >= T_POLLB) break; __builtin_amdgcn_s_sleep(4); }
    {
      const int s2 = (c + 4 * g) & 7;
      int vo2 = 3072 + s2 * 128;
      int lw = S_Y + (a * 128 + c * 8 + g) * 4;
      int ow = S_O + a * 1904 + (lane >> 5) * 272 + (lane & 31) * 8;
      int orr = S_O + a * 1904 + (c < 6 ? c : 6) * 272 + g * 16;
      *(LAS v2u*)(L + S_O + a * 1904 + 6 * 272 + (lane & 31) * 8) = v2u{0u, 0u};
      PIN(vo2); PIN(lw); PIN(ow); PIN(orr);
      v4u oa[3], ml[1];
      ml[0] = v4u{0xff800000u, elo, 0u, ehi};
      bool b0 = true, b1 = true, b2 = true, b3 = s2 < 6 && lane < 32 && c < 8;
#pragma nounroll
      for (int it = 0; it < SPIN; ++it) {
        CBAR();
        if (b3) ml[0] = LD16(rws, vo2, WS_OP + hb * 3840, SC1);
        if (b0) oa[0] = LD16(rws, lane * 16, WS_OP + hb * 3840, SC1);
        if (b1) oa[1] = LD16(rws, lane * 16, WS_OP + hb * 3840 + 1024, SC1);
        if (b2) oa[2] = LD16(rws, lane * 16, WS_OP + hb * 3840 + 2048, SC1);
        b3 = badtags<1>(ml, elo, ehi) != 0u;
        b0 = ((oa[0].y ^ elo) | (oa[0].w ^ ehi)) != 0u; b1 = ((oa[1].y ^ elo) | (oa[1].w ^ ehi)) != 0u; b2 = ((oa[2].y ^ elo) | (oa[2].w ^ ehi)) != 0u;
        if (none(b0 | b1 | b2 | b3)) break;
      }
#pragma unroll
      for (int u = 0; u < 3; ++u) *(LAS v2u*)(L + ow + u * 544) = v2u{oa[u].x, oa[u].z};
      CBAR();
      v4u af[4];
#pragma unroll
      for (int ks = 0; ks < 4; ++ks) af[ks] = *(const LAS v4u*)(L + orr + ks * 64);
      f32x4 d[8];
#pragma unroll
      for (int t = 0; t < 8; ++t) d[t] = f32x4{0.f, 0.f, 0.f, 0.f};
#pragma unroll
      for (int ks = 0; ks < 4; ++ks)
#pragma unroll
        for (int t = 0; t < 8; ++t) d[t] = mfma(af[ks], BF[ks][t], d[t]);
      const float ms = U2F(ml[0].x);
      float M = fmaxf(ms, dpp<XOR1>(ms)); M = fmaxf(M, dpp<XOR2>(M)); M = fmaxf(M, dpp<XOR7>(M));
      const float e = U2F(ml[0].z) * __expf(ms - M);
      float Ls = e + dpp<XOR1>(e); Ls += dpp<XOR2>(Ls); Ls += dpp<XOR7>(Ls);
      const float wt = lane < 32 ? e * __builtin_amdgcn_rcpf(Ls) : 0.f;
      const float w0 = dpp<BCAST + 0>(wt), w1_ = dpp<BCAST + 1>(wt), w2_ = dpp<BCAST + 2>(wt), w3 = dpp<BCAST + 3>(wt);
      float y[8];
#pragma unroll
      for (int t = 0; t < 8; ++t) y[t] = fmaf(d[t].w, w3, fmaf(d[t].z, w2_, fmaf(d[t].y, w1_, d[t].x * w0)));
      const float ya = sw32(sw16(y[0], y[1]), sw16(y[2], y[3])), yb = sw32(sw16(y[4], y[5]), sw16(y[6], y[7]));
      *(LAS float*)(L + lw) = ya; *(LAS float*)(L + lw + 16) = yb;
      CBAR();
      if (lane == 0) __hip_atomic_fetch_add(FC + G_Y, 1u, __ATOMIC_RELAXED, __HIP_MEMORY_SCOPE_WORKGROUP);
      CBAR();
      if (a == 0) {
        const volatile LAS f32x2* tp = (const volatile LAS f32x2*)(L + S_Y) + lane;
        f32x2 t0, t1, t2, t3;
#pragma nounroll
        for (int it = 0; it < SPIN * 8; ++it) {
          const unsigned cnt = FL[G_Y];
          t0 = tp[0]; t1 = tp[64]; t2 = tp[128]; t3 = tp[192];
          if (__builtin_amdgcn_readfirstlane(cnt) >= 4u) break;
        }
        const f32x2 yy = (t0 + t1) + (t2 + t3);
        ST8((v2u{nosentf(yy.x), nosentf(yy.y)}), rws, lane * 8, WS_YP + xg * 2560 + k * 512, SC1);
      }
    }
    if (role_d) {
      v4u yq;
#pragma nounroll
      for (int it = 0; it < SPIN; ++it) {
        CBAR();
        yq = LD16(rws, q4 * 16 + (part & 7) * 2560, WS_YP + v * 64, SC1);
        if (none(mx4(yq) == SENT)) break;
        __builtin_amdgcn_s_sleep(2);
      }
      const unsigned long long ty = __builtin_amdgcn_s_memrealtime();
      auto red = [&](f32x4 t) { float s = red4(t); s += dpp<ROR4>(s); s += dpp<ROR8>(s); return s; };
      float x1 = rbf(dx + rbf(dga * rbf(0.5f * red(F4(yq)))));
      int o0 = q4 * 16 + part * 2560;
      PIN(x1); PIN(o0);
      v4u f0, f1 = {0u, 0u, 0u, 0u};
      bool b0 = true, b1 = part < 11;
#pragma nounroll
      for (int it = 0; it < SPIN; ++it) { if (__builtin_amdgcn_s_memrealtime() - ty >= T_D) break; }
#pragma nounroll
      for (int it = 0; it < SPIN; ++it) {
        CBAR();
        if (b0) f0 = LD16(rws, o0, WS_FP + v * 64, SC1);
        if (b1) f1 = LD16(rws, o0, WS_FP + v * 64 + 16 * 2560, SC1);
        b0 = mx4(f0) == SENT; b1 = mx4(f1) == SENT;
        if (none(b0 | b1)) break;
      }
      const float mo = red(F4(f0) + F4(f1));
      const float res = x1 + rbf(dgb * rbf(mo));
      if (c < 4) ((__bf16*)out)[v * 16 + q4 * 4 + g] = (__bf16)res;
      if (v == 0 && lane == 0) ST8((v2u{elo, ehi}), rws, 0, WS_EPOCH, SC1);
      asm volatile(".Lpf_end2:");
    }
    return;
  }

  float piv;
  {
    const int u1 = (k >= 1 && k <= 5) ? xg * 5 + k - 1 : (k == 0 ? 72 + xg * 2 : 48 + xg * 4 + (k - 6) * 2);
    const int u2 = role_q ? 88 + xg * 4 + k - 1 : (k == 5 ? 40 + xg : u1 + 1);
    const rsrc_t rm = mkrs(wmod, D * 6 * D * 2);
    const int mvo = (10 * (2 * g + rho)) * 7680 + chi * 16;
    v4u WM[10];
    const v4u mk = selmask(c);
    auto mod_issue = [&](int um) {
#pragma unroll
      for (int i = 0; i < 10; ++i) WM[i] = LD16(rm, mvo, (320 * (um & 1) + 80 * a + i) * 7680 + (um >> 1) * 128, 0);
      SB(); CBAR();
    };
    auto mod_unit = [&](int u, int cnt, int um) {
      const LAS v2u* sp_ = (const LAS v2u*)(L + S_S) + (um & 1) * 160 + a * 40 + 5 * (2 * g + rho);
      v2u sv2[5];
#pragma unroll
      for (int i = 0; i < 5; ++i) sv2[i] = sp_[i];
      f32x4 acc = {0.f, 0.f, 0.f, 0.f};
#pragma unroll
      for (int i = 0; i < 5; ++i) { acc = mfma(selfrag(sv2[i].x, mk), WM[2 * i], acc); acc = mfma(selfrag(sv2[i].y, mk), WM[2 * i + 1], acc); }
      if ((g >> 1) == rho) *(LAS f32x4*)(L + S_MB + u * 2048 + ((a * 2 + rho) * 16 + chi * 2 + (g & 1)) * 16) = acc;
      lds_arrive(FC + cnt, lane);
      if (a == 0) {
        lds_wait(FL + cnt, 4);
        const LAS f32x2* mb = (const LAS f32x2*)(L + S_MB + u * 2048) + (lane & 31);
        const f32x2 mv = ((mb[0] + mb[32]) + (mb[64] + mb[96])) + ((mb[128] + mb[160]) + (mb[192] + mb[224]));
        ST16((v4u{F2U(mv.x), elo, F2U(mv.y), ehi}), rws, lane < 32 ? lane * 16 : OOB, WS_MP + (um & 1) * 30720 + (um >> 1) * 512, SC1);
        lds_arrive(FC + cnt, lane);
      }
    };
    FL[lane] = 0u;
    v2u ep = ld_epoch(rws, lane);
    unsigned xr[5];
#pragma unroll
    for (int i = 0; i < 5; ++i) xr[i] = LD4(rx, lane * 4, i * 256, 0);
    const unsigned xa = LD4(rx, lane * 4, a * 256, 0), xb = LD4(rx, c * 4, 1024 + a * 64, 0);
    if (role_m) {
      const v2u cv = LD8(mkrs(cvec, D * 2), lane < 40 ? (lane >= 20 ? 640 + (lane - 20) * 8 : lane * 8) : OOB, a * 160, 0);
      mod_issue(u1);
      BARRIER();
      float cf[4] = {bflo(cv.x), bfhi(cv.x), bflo(cv.y), bfhi(cv.y)};
      unsigned sd[4];
#pragma unroll
      for (int i = 0; i < 4; ++i) { const float sv = cf[i] / (1.f + __expf(-cf[i])); sd[i] = cvtpk(sv, sv); }
      if (lane < 40) ((LAS v4u*)(L + S_S))[(lane >= 20 ? 60 + lane : lane) + a * 20] = v4u{sd[0], sd[1], sd[2], sd[3]};
      LGKM0();
      PIN(ep);
      EPOCH_TAG(ep, elo, ehi);
      mod_unit(0, G_M1, u1);
    } else {
      BARRIER();
      PIN(ep);
      EPOCH_TAG(ep, elo, ehi);
    }
    {
      float s = 0.f;
#pragma unroll
      for (int i = 0; i < 5; ++i) s += bflo(xr[i]) + bfhi(xr[i]);
      piv = wsum(s) * (1.f / D);
    }
    if (a == 3) {
      const v4u sv = {SENT, SENT, SENT, SENT};
      ST16(sv, rws, (role_b && lane < 32) ? lane * 16 : OOB, WS_YP + xg * 2560 + k * 512, SC1);
      if (role_c) { ST16(sv, rws, lane * 16, WS_FP + uc * 2560, SC1); ST16(sv, rws, lane * 16, WS_FP + uc * 2560 + 1024, SC1); ST16(sv, rws, lane < 32 ? lane * 16 : OOB, WS_FP + uc * 2560 + 2048, SC1); }
    }
    if (role_q) {
      float mean = piv;
      float v = 0.f;
#pragma unroll
      for (int i = 0; i < 5; ++i) { const float d0 = bflo(xr[i]) - mean, d1 = bfhi(xr[i]) - mean; v += d0 * d0 + d1 * d1; }
      v = wsum(v) * (1.f / D);
      const float den = rbf(sqrtf(rbf(rbf(v) + 1e-5f)));
      mean = rbf(mean);
      float n0 = rbf(rbf(bflo(xa) - mean) / den), n1 = rbf(rbf(bfhi(xa) - mean) / den), n2 = rbf(rbf(bflo(xb) - mean) / den), n3 = rbf(rbf(bfhi(xb) - mean) / den);
      PIN(n0); PIN(n1); PIN(n2); PIN(n3);
      v4u u[5];
      bool bd = true;
#pragma nounroll
      for (int it = 0; it < SPIN * 4; ++it) { if (__builtin_amdgcn_s_memrealtime() - tstart >= T_POLL1) break; __builtin_amdgcn_s_sleep(1); }
#pragma nounroll
      for (int it = 0; it < SPIN; ++it) {
        CBAR();
        if (bd) {
#pragma unroll
          for (int i = 0; i < 4; ++i) u[i] = LD16(rws, lane * 16, WS_MP + (i & 1) * 30720 + (i >> 1) * 5120 + a * 1024, SC1);
          u[4] = LD16(rws, (g & 1) * 30720 + (g >> 1) * 5120 + c * 16, WS_MP + 4096 + a * 256, SC1);
          bd = badtags<5>(u, elo, ehi) != 0u;
        }
        if (none(bd)) break;
      }
      const float sa0 = rbf(U2F(u[0].x) + U2F(u[1].x)), sa1 = rbf(U2F(u[0].z) + U2F(u[1].z)), ca0 = rbf(U2F(u[2].x) + U2F(u[3].x)), ca1 = rbf(U2F(u[2].z) + U2F(u[3].z));
      const float h0 = rbf(n0 * rbf(1.f + ca0)) + sa0, h1 = rbf(n1 * rbf(1.f + ca1)) + sa1;
      ((LAS v2u*)(L + S_HT))[a * 64 + lane] = v2u{cvtpk(h0, h0), cvtpk(h1, h1)};
      const float e0 = rbf(x16sum(U2F(u[4].x))), e1 = rbf(x16sum(U2F(u[4].z)));
      auto s0 = __builtin_amdgcn_permlane32_swap(F2U(e0), F2U(e0), false, false); auto s1 = __builtin_amdgcn_permlane32_swap(F2U(e1), F2U(e1), false, false);
      const float h2 = rbf(n2 * rbf(1.f + U2F(s0[1]))) + U2F(s0[0]), h3 = rbf(n3 * rbf(1.f + U2F(s1[1]))) + U2F(s1[0]);
      if (lane < 16) ((LAS v2u*)(L + S_HT))[256 + a * 16 + lane] = v2u{cvtpk(h2, h2), cvtpk(h3, h3)};
      lds_arrive(FC + G_H, lane);
      lds_wait(FL + G_H, 4);
    }
    if (role_m) { mod_issue(u2); mod_unit(1, G_M2, u2); }
    if (a == 3) {
      VMW(0);
      ST16((v4u{0u, elo, 0u, ehi}), rws, lane == 0 ? 0 : OOB, WS_FL + (xg * 8 + k) * 128, SC1);
      FL[G_ARM] = 1u;
    }
  }
  if (!role_c) return;
  typedef short v4s __attribute__((ext_vector_type(4)));
#define TRRD(p_) __builtin_bit_cast(v2u, __builtin_amdgcn_ds_read_tr16_b64_v4i16((LAS v4s*)(p_)))
  v4u B1[5][4];
  v4u B2[2][10];
  const int q_ = c >> 2, p_ = c & 3;
  lds_wait(FL + G_W1, 4);
  {
    const int f_ = (q_ >> 1) | ((g & 1) << 1) | ((p_ & 1) << 2);
    LAS char* wi = L + S_W1 + (160 * p_ + 8 * g + q_) * 128;
#pragma unroll
    for (int t = 0; t < 4; ++t) {
      LAS char* wt = wi + (((2 * a + (t >> 1)) ^ f_) * 16) + (t & 1) * 8;
#pragma unroll
      for (int ks = 0; ks < 5; ++ks) { const v2u lo = TRRD(wt + ks * 4096), hi = TRRD(wt + ks * 4096 + 512); B1[ks][t] = v4u{lo.x, lo.y, hi.x, hi.y}; }
    }
#pragma unroll
    for (int ks = 0; ks < 5; ++ks)
#pragma unroll
      for (int t = 0; t < 4; ++t) PIN(B1[ks][t]);
  }
  lds_arrive(FC + G_CV, lane);
  const int el = 32 * a + 4 * chi + g;
  float fa[5], fb[5], fm[5], fn[5], Cc, Ss;
  const LAS char* zr = L + S_Z + p_ * 320 + g * 16;
  {
    unsigned xe[5];
#pragma unroll
    for (int i = 0; i < 5; ++i) xe[i] = LD4(rx, (el >> 1) * 4, i * 256, 0);
    v4u mm[9];
    const int c3 = a < 3 ? a + 12 : a;
    bool bd = true, bt = lane < 16;
    mm[8] = v4u{0u, elo, 0u, ehi};
#pragma nounroll
    for (int it = 0; it < SPIN; ++it) {
      CBAR();
      if (bd) {
#pragma unroll
        for (int i = 0; i < 8; ++i) mm[i] = LD16(rws, lane * 16, WS_MP + 10240 + (i & 1) * 30720 + (i < 6 ? a + 4 * (i >> 1) : c3) * 1024, SC1);
      }
      if (bt) mm[8] = LD16(rws, lane * 128, WS_FL + a * 2048, SC1);
      bd = badtags<9>(mm, elo, ehi) != 0u; bt = bt && bd;
      if (none(bd)) break;
      __builtin_amdgcn_s_sleep(16);
    }
#pragma unroll
    for (int i = 0; i < 4; ++i) *(LAS f32x2*)(L + S_MV + (i < 3 ? a + 4 * i : c3) * 512 + lane * 8) = f32x2{rbf(U2F(mm[2 * i].x) + U2F(mm[2 * i + 1].x)), rbf(U2F(mm[2 * i].z) + U2F(mm[2 * i + 1].z))};
    lds_arrive(FC + G_P1, lane);
    lds_wait(FL + G_P1, 4);
#pragma unroll
    for (int i = 0; i < 5; ++i) {
      const LAS float* mv = (const LAS float*)(L + S_MV) + el + 128 * i;
      const float gav = mv[0], sbv = mv[640], c1 = rbf(1.f + mv[1280]);
      fa[i] = gav; fb[i] = (g & 1) ? bfhi(xe[i]) : bflo(xe[i]); fm[i] = rho ? 1.f : c1; fn[i] = -piv * fm[i];
      *(LAS __bf16*)(L + S_Z + rho * 1280 + (el + 128 * i) * 2) = (__bf16)(rho ? sbv : c1);
      PIN(fa[i]); PIN(fb[i]); PIN(fm[i]); PIN(fn[i]);
    }
    lds_arrive(FC + G_P2, lane);
    lds_wait(FL + G_P2, 4);
    f32x4 dc[4], ds[4];
#pragma unroll
    for (int t = 0; t < 4; ++t) { dc[t] = f32x4{0.f, 0.f, 0.f, 0.f}; ds[t] = dc[t]; }
#pragma unroll
    for (int ks = 0; ks < 5; ++ks) {
      const v4u ac = *(const LAS v4u*)(zr + ks * 64), as = *(const LAS v4u*)(zr + 1280 + ks * 64);
#pragma unroll
      for (int t = 0; t < 4; ++t) { dc[t] = mfma(ac, B1[ks][t], dc[t]); ds[t] = mfma(as, B1[ks][t], ds[t]); }
    }
    const bool g1 = g & 1, g2 = g & 2;
#define SEL4(d_) f32x4{g2 ? (g1 ? d_[3].x : d_[2].x) : (g1 ? d_[1].x : d_[0].x), g2 ? (g1 ? d_[3].y : d_[2].y) : (g1 ? d_[1].y : d_[0].y), \
                       g2 ? (g1 ? d_[3].z : d_[2].z) : (g1 ? d_[1].z : d_[0].z), g2 ? (g1 ? d_[3].w : d_[2].w) : (g1 ? d_[1].w : d_[0].w)}
#pragma unroll
    for (int t = 0; t < 4; ++t) { PIN(dc[t]); PIN(ds[t]); }
    { const f32x4 u = SEL4(dc), v = SEL4(ds); Cc = FOLD4(u); Ss = FOLD4(v); }
    PIN(Cc); PIN(Ss);
    lds_arrive(FC + G_P3, lane);
  }
  lds_wait(FL + G_W2, 4);
  {
    const int f2 = (q_ | ((g & 1) << 2)) << 1;
#pragma unroll
    for (int t = 0; t < 10; ++t) {
      const int cb = 320 * a + 32 * t + 8 * p_;
      const int cs = (cb & ~255) | ((((cb >> 4) & 15) ^ f2) << 4) | (cb & 8);
#pragma unroll
      for (int ks = 0; ks < 2; ++ks) {
        v2u h[2];
#pragma unroll
        for (int rd = 0; rd < 2; ++rd) { const int o = (32 * ks + 8 * g + 4 * rd + q_) * 1280 + cs; h[rd] = TRRD(L + (o < 49152 ? S_W2 + o : S_W1 + o - 49152)); }
        B2[ks][t] = v4u{h[0].x, h[0].y, h[1].x, h[1].y};
      }
      PIN(B2[0][t]); PIN(B2[1][t]);
    }
  }
  lds_wait(FL + G_P3, 4);
  {
    int zw = (rho ? S_X1 : S_Z) + el * 2;
    int hw = c < 4 ? S_H + (a * 16 + 4 * g + c) * 2 : S_ST + 16;
    int yo = (lane >> 3) * 2560 + a * 128 + (lane & 7) * 16;
    int st3 = lane < 32 ? lane * 4 + 512 : OOB;
    PIN(zw); PIN(hw); PIN(yo); PIN(st3);
#pragma nounroll
    for (int it = 0; it < SPIN * 4; ++it) { if (__builtin_amdgcn_s_memrealtime() - tstart >= T_POLLB) break; __builtin_amdgcn_s_sleep(4); }
    v4u yp[5];
    bool bd = true;
#pragma nounroll
    for (int it = 0; it < SPIN; ++it) {
      CBAR();
      if (bd) {
#pragma unroll
        for (int i = 0; i < 5; ++i) yp[i] = LD16(rws, yo, WS_YP + i * 512, SC1);
      }
      bd = umax(umax(umax(mx4(yp[0]), mx4(yp[1])), umax(mx4(yp[2]), mx4(yp[3]))), mx4(yp[4])) == SENT;
      if (none(bd)) break;
    }
#pragma unroll
    for (int i = 0; i < 5; ++i) {
      float at = red4(F4(yp[i]));
      at += dpp<ROR8>(at);
      *(LAS __bf16*)(L + zw + 256 * i) = (__bf16)fmaf(rbf(fmaf(fa[i], at, fb[i])), fm[i], fn[i]);
    }
    BARRIER();
    v4u af[5];
#pragma unroll
    for (int ks = 0; ks < 5; ++ks) af[ks] = *(const LAS v4u*)(zr + ks * 64);
    const v4u xa = *(const LAS v4u*)(L + S_X1 + lane * 16);
    const unsigned xb = *(const LAS unsigned*)(L + S_X1 + 1024 + lane * 4);
    SB();
    f32x4 d[4];
#pragma unroll
    for (int t = 0; t < 4; ++t) d[t] = f32x4{0.f, 0.f, 0.f, 0.f};
#pragma unroll
    for (int ks = 0; ks < 5; ++ks)
#pragma unroll
      for (int t = 0; t < 4; ++t) d[t] = mfma(af[ks], B1[ks][t], d[t]);
    const unsigned one2 = 0x3f803f80u;
    const float t1 = dot2(xb, one2, dot2(xa.x, one2, dot2(xa.y, one2, 0.f))) + dot2(xa.z, one2, dot2(xa.w, one2, 0.f));
    const float t2 = dot2(xb, xb, dot2(xa.x, xa.x, dot2(xa.y, xa.y, 0.f))) + dot2(xa.z, xa.z, dot2(xa.w, xa.w, 0.f));
    const float s12 = x32sum(rsum(sw16(t1, t2)));
    const float mean = U2F(__builtin_amdgcn_readlane(F2U(s12), 0)) * (1.f / D);
    const float var = fmaxf(U2F(__builtin_amdgcn_readlane(F2U(s12), 16)) * (1.f / D) - mean * mean, 0.f);
    const float rden = __builtin_amdgcn_rcpf(rbf(__builtin_amdgcn_sqrtf(rbf(rbf(var) + 1e-5f))));
    float tt = Ss - Cc * ((rbf(mean + piv) - piv) * rden);
    PIN(tt);
    const bool g1 = g & 1, g2 = g & 2;
#pragma unroll
    for (int t = 0; t < 4; ++t) PIN(d[t]);
    const f32x4 dd = SEL4(d);
    *(LAS __bf16*)(L + hw) = (__bf16)fmaxf(fmaf(FOLD4(dd), rden, tt), 0.f);
    BARRIER();
    const v4u ha0 = *(const LAS v4u*)(L + S_H + g * 16), ha1 = *(const LAS v4u*)(L + S_H + 64 + g * 16);
    f32x4 o[10];
#pragma unroll
    for (int t = 0; t < 10; ++t) o[t] = mfma(ha1, B2[1][t], mfma(ha0, B2[0][t], f32x4{0.f, 0.f, 0.f, 0.f}));
    float ov[10];
#pragma unroll
    for (int t = 0; t < 10; ++t) { ov[t] = o[t].x; PIN(ov[t]); }
    ST4(nosentf(g2 ? (g1 ? ov[3] : ov[2]) : (g1 ? ov[1] : ov[0])), rws, lane * 4, WS_FP + uc * 2560 + a * 640, SC1);
    ST4(nosentf(g2 ? (g1 ? ov[7] : ov[6]) : (g1 ? ov[5] : ov[4])), rws, lane * 4, WS_FP + uc * 2560 + a * 640 + 256, SC1);
    ST4(nosentf(g1 ? ov[9] : ov[8]), rws, st3, WS_FP + uc * 2560 + a * 640, SC1);
  }
  asm volatile(".Lpf_end:");
}

extern "C" void kernel_launch(void* const* d_in, const int* in_sizes, int n_in, void* d_out, int out_size, void* d_ws, size_t ws_size, hipStream_t stream) {
  fused_kernel<<<256, 512, 0, stream>>>((const u16*)d_in[1], (const u16*)d_in[2], (char*)d_ws, (const u16*)d_in[9], (const u16*)d_in[8], (const u16*)d_in[0],
      (const u16*)d_in[3], (const u16*)d_in[4], (const u16*)d_in[6], (const u16*)d_in[7], (u16*)d_out);
}
```
